```python
import torch

def _softmax(x, axis=-1):
    return torch.softmax(x, dim=axis)
def _gelu(x):
    return torch.nn.functional.gelu(x, approximate="tanh")
def _silu(x):
    return torch.nn.functional.silu(x)
def _sigmoid(x):
    return torch.sigmoid(x)
def _layernorm(x, axis=-1, eps=1e-5):
    m = x.mean(dim=axis, keepdim=True)
    v = x.var(dim=axis, keepdim=True, unbiased=False)
    return (x - m) / torch.sqrt(v + eps)
def _scatter_add(tgt, idx, src, axis=0):
    return tgt.clone().index_add_(axis, idx, src)
def _segment_sum(src, seg, n):
    out = torch.zeros((n,) + src.shape[1:], dtype=src.dtype)
    return out.index_add_(0, seg, src)
def _pad(x, pw):
    flat = tuple(v for pair in reversed(pw) for v in pair)
    return torch.nn.functional.pad(x, flat)

kernel_name = 'standalone__paged_block_table_kv_radixattention__sampling'

def setup_inputs():

    _w = (lambda a: (lambda t: t.to(torch.bfloat16) if t.is_floating_point() else t)(torch.as_tensor(a)))
    B, H, Hkv, Dh = 32, 16, 8, 256
    Lk = 2048
    bs = 16
    nb = max(2, Lk // bs)
    P = max(1, nb // 2)
    n_phys = P + B * (nb - P)
    shared = torch.arange(P, dtype=torch.int64)
    priv = P + torch.arange(B * (nb - P), dtype=torch.int64).reshape(B, nb - P)
    bt = torch.cat([torch.tile(shared[None, :], (B, 1)), priv], dim=1)
    ctx = nb * bs - (torch.arange(1, B + 1, dtype=torch.int64) * 2654435761) % max(1, (nb * bs - P * bs) // 2)
    return {
        "q": _w((lambda *s: torch.randn(*s, dtype=torch.bfloat16) * 0.8)(B, H, Dh)),
        "k_cache": _w((lambda *s: torch.randn(*s, dtype=torch.bfloat16) * 0.8)(n_phys, bs, Hkv, Dh)),
        "v_cache": _w((lambda *s: torch.randn(*s, dtype=torch.bfloat16) * 0.05)(n_phys, bs, Hkv, Dh)),
        "block_table": _w(bt.to(torch.int64)),
        "ctx_lens": _w(torch.as_tensor(ctx).to(torch.int64)),
    }

def reference(q, k_cache, v_cache, block_table, ctx_lens):
    B, H, Dh = q.shape
    n_phys, bs, Hkv, _ = k_cache.shape
    nb = block_table.shape[1]
    Lk = nb * bs
    rep = H // Hkv
    k = k_cache[block_table].reshape(B, Lk, Hkv, Dh)
    v = v_cache[block_table].reshape(B, Lk, Hkv, Dh)
    k = (lambda x, n, axis: torch.repeat_interleave(x, n, dim=axis))(k, rep, axis=2)
    v = (lambda x, n, axis: torch.repeat_interleave(x, n, dim=axis))(v, rep, axis=2)
    scores = torch.einsum("bhd,bkhd->bhk", q, k) * (Dh ** -0.5)
    pos = torch.arange(Lk)
    valid = pos[None, :] < ctx_lens[:, None]
    scores = torch.where(valid[:, None, :], scores, -1e9)
    p = _softmax(scores, axis=-1)
    return torch.einsum("bhk,bkhd->bhd", p, v)
```

```cpp
#include <hip/hip_runtime.h>
#include <hip/hip_bf16.h>
#include <cstdint>

#define SGRP 8
#define WSH 4
#define KV_NT 1

namespace {
constexpr int B = 32, H = 16, HKV = 8, D = 256, BS = 16, NB = 128;
constexpr int S = SGRP, NG = B / S;
constexpr int NWG = 32;
constexpr int NW = 8, NTHR = NW * 64;
constexpr int WPRIV = 4, WSHARED = WSH;
constexpr int NSEG = NG + B;
constexpr float C2 = 0.0625f * 1.4426950408889634f;
constexpr int PAGE_ELEMS = BS * HKV * D;
constexpr int TOK_STRIDE = HKV * D;

constexpr size_t WS_PLAN = 0;
constexpr size_t WS_ML_SH = 4096;
constexpr size_t WS_ML_PR = WS_ML_SH + (size_t)HKV * NG * NWG * 2 * S * 8;
constexpr size_t WS_PO_SH = WS_ML_PR + (size_t)HKV * B * NWG * 2 * 8;
constexpr size_t WS_PO_PR = WS_PO_SH + (size_t)HKV * NG * NWG * 2 * S * D * 4;
constexpr size_t WS_END = WS_PO_PR + (size_t)HKV * B * NWG * 2 * D * 4;

constexpr int QH_STRIDE = 576;
constexpr int QS_STRIDE = 2 * QH_STRIDE;
constexpr int LDS_BT = 0;
constexpr int LDS_Q = LDS_BT + B * NB * 4;
constexpr int MR = 4;
constexpr int MREC = 264;
constexpr int LDS_MRG = LDS_Q + B * QS_STRIDE;
constexpr int LDS_MISC = LDS_MRG + NW * MR * 2 * MREC * 4;
constexpr int LDS_BYTES = LDS_MISC + 64;

using bf16x8 = __attribute__((ext_vector_type(8))) __bf16;
using u32x4 = __attribute__((ext_vector_type(4))) unsigned;
using i32x4 = __attribute__((ext_vector_type(4))) int;
using f32x4 = __attribute__((ext_vector_type(4))) float;

__device__ __forceinline__ u32x4 ldkv(const void* p) {
  return __builtin_nontemporal_load(reinterpret_cast<const u32x4*>(p));
}
__device__ __forceinline__ f32x4 mfma(u32x4 a, u32x4 b, f32x4 c) {
  return __builtin_amdgcn_mfma_f32_16x16x32_bf16(__builtin_bit_cast(bf16x8, a), __builtin_bit_cast(bf16x8, b), c, 0, 0, 0);
}
__device__ __forceinline__ unsigned cvtpk(float lo, float hi) {
  using f32x2 = __attribute__((ext_vector_type(2))) float;
  using bf16x2 = __attribute__((ext_vector_type(2))) __bf16;
  const f32x2 v = {lo, hi};
  return __builtin_bit_cast(unsigned, __builtin_convertvector(v, bf16x2));
}
template <int CTRL> __device__ __forceinline__ float dpp(float x) {
  return __builtin_bit_cast(float, __builtin_amdgcn_mov_dpp(__builtin_bit_cast(int, x), CTRL, 0xf, 0xf, true));
}
__device__ __forceinline__ float xkg_max(float x) {
  auto s = __builtin_amdgcn_permlane16_swap(__float_as_uint(x), __float_as_uint(x), false, false);
  x = fmaxf(__uint_as_float(s[0]), __uint_as_float(s[1]));
  auto t = __builtin_amdgcn_permlane32_swap(__float_as_uint(x), __float_as_uint(x), false, false);
  return fmaxf(__uint_as_float(t[0]), __uint_as_float(t[1]));
}
__device__ __forceinline__ float xkg_sum(float x) {
  auto s = __builtin_amdgcn_permlane16_swap(__float_as_uint(x), __float_as_uint(x), false, false);
  x = __uint_as_float(s[0]) + __uint_as_float(s[1]);
  auto t = __builtin_amdgcn_permlane32_swap(__float_as_uint(x), __float_as_uint(x), false, false);
  return __uint_as_float(t[0]) + __uint_as_float(t[1]);
}
__device__ __forceinline__ int wave_incl_scan(int x, int lane) {
#pragma unroll
  for (int o = 1; o < 64; o <<= 1) {
    int y = __shfl_up(x, o, 64);
    if (lane >= o) x += y;
  }
  return x;
}
__device__ __forceinline__ int live_pages(int ctx) {
  if (ctx <= 0) return NB;
  ctx = min(ctx, NB * BS);
  return (ctx + BS - 1) / BS;
}
__device__ __forceinline__ void seg_geometry(int lane, int ctx_of_lane_minus_ng, int Pc, int& npg, int& wt, int& wstart, int& wtotal) {
  npg = 0; wt = WPRIV;
  if (lane < NG) { npg = Pc; wt = WSHARED; }
  else if (lane < NSEG) { npg = live_pages(ctx_of_lane_minus_ng) - Pc; }
  const int wl = npg * wt;
  const int inc = wave_incl_scan(wl, lane);
  wstart = inc - wl;
  wtotal = __shfl(inc, 63, 64);
}
__device__ __forceinline__ int pos_to_page(int pos, int wstart, int wt, int npg) {
  int d = pos - wstart;
  if (d <= 0) return 0;
  return min((d + wt - 1) / wt, npg);
}

struct KV { u32x4 k[8]; u32x4 v[8]; };

__device__ __forceinline__ void issue_k(KV& r, const __hip_bfloat16* kbase, int phys) {
  const __hip_bfloat16* p = kbase + (size_t)phys * PAGE_ELEMS;
#pragma unroll
  for (int c = 0; c < 8; ++c) r.k[c] = ldkv(p + c * 32);
}
__device__ __forceinline__ void issue_v(KV& r, const __hip_bfloat16* vbase, int phys) {
  const __hip_bfloat16* p = vbase + (size_t)phys * PAGE_ELEMS;
#pragma unroll
  for (int i = 0; i < 4; ++i)
#pragma unroll
    for (int h = 0; h < 2; ++h) r.v[i * 2 + h] = ldkv(p + i * TOK_STRIDE + h * 128);
}

struct Ctx {
  const __hip_bfloat16* kbase; const __hip_bfloat16* vbase;
  unsigned char* lds; unsigned char* ws;
  int lane, wave, head, wgi, plist, nitems;
  unsigned mk[4];
};

template <int NS>
__device__ __forceinline__ void run_segment(const Ctx& cx, KV& r, int seg, int seq0, int kb, int ke, int pg0, int ctxlen) {
  const int lane = cx.lane;
  float m[NS], l[NS];
  f32x4 acc[NS][2];
#pragma unroll
  for (int s = 0; s < NS; ++s) { m[s] = -INFINITY; l[s] = 0.f; acc[s][0] = f32x4{0.f, 0.f, 0.f, 0.f}; acc[s][1] = f32x4{0.f, 0.f, 0.f, 0.f}; }
  int qoff = LDS_Q + seq0 * QS_STRIDE + ((lane >> 3) & 1) * QH_STRIDE + (lane >> 4) * 16;
  const bool uni = (NS == 1) && (ctxlen <= 0);
  const bool samehead = ((lane >> 5) & 1) == ((lane >> 3) & 1);
  for (int k = kb; k < ke; ++k) {
    const int pg = pg0 + 8 * (k - kb);
    const bool more = (k + 1) < cx.nitems;
    const int nphys = __builtin_amdgcn_readlane(cx.plist, more ? k + 1 : k);
    f32x4 sc[NS];
#pragma unroll
    for (int s = 0; s < NS; ++s) {
      f32x4 a = f32x4{0.f, 0.f, 0.f, 0.f};
      if (NS > 1) asm volatile("" : "+v"(qoff));
#pragma unroll
      for (int c = 0; c < 8; ++c) {
        const u32x4 qf = *reinterpret_cast<const u32x4*>(cx.lds + qoff + s * QS_STRIDE + c * 64);
        a = mfma(r.k[c], qf, a);
      }
      sc[s] = a;
      if (NS > 1) __builtin_amdgcn_sched_barrier(0);
    }
    if (more) issue_k(r, cx.kbase, nphys);
    int rem = BS;
    if (NS == 1 && !uni) rem = ctxlen - pg * BS;
#pragma unroll
    for (int s = 0; s < NS; ++s) {
      float t[4];
#pragma unroll
      for (int i = 0; i < 4; ++i) t[i] = sc[s][i] * C2;
      if (NS == 1) {
        if (uni) { t[0] = t[1] = t[2] = t[3] = 0.f; }
        if (rem < BS) {
#pragma unroll
          for (int i = 0; i < 4; ++i) t[i] = ((lane >> 4) * 4 + i < rem) ? t[i] : -INFINITY;
        }
      }
      float pm = fmaxf(fmaxf(t[0], t[1]), fmaxf(t[2], t[3]));
      pm = xkg_max(pm);
      const float mn = fmaxf(m[s], pm);
      const float msub = (mn == -INFINITY) ? 0.f : mn;
      const float alpha = __builtin_amdgcn_exp2f(m[s] - msub);
      m[s] = mn;
      unsigned pp[4];
      float ps = 0.f;
#pragma unroll
      for (int i = 0; i < 4; ++i) {
        const float p = __builtin_amdgcn_exp2f(t[i] - msub);
        pp[i] = cvtpk(p, p);
        ps += __uint_as_float(pp[i] & 0xffff0000u);
      }
      l[s] = fmaf(l[s], alpha, ps);
      const float aoth = dpp<0x128>(alpha);
      const float aa = samehead ? alpha : aoth;
      f32x4 a0 = acc[s][0] * aa, a1 = acc[s][1] * aa;
#pragma unroll
      for (int i = 0; i < 4; ++i) {
        const u32x4 pa = u32x4{pp[i] & cx.mk[0], pp[i] & cx.mk[1], pp[i] & cx.mk[2], pp[i] & cx.mk[3]};
        a0 = mfma(pa, r.v[i * 2 + 0], a0);
        a1 = mfma(pa, r.v[i * 2 + 1], a1);
      }
      acc[s][0] = a0; acc[s][1] = a1;
      if (NS > 1) __builtin_amdgcn_sched_barrier(0);
    }
    if (more) issue_v(r, cx.vbase, nphys);
  }
  float* mrg = reinterpret_cast<float*>(cx.lds + LDS_MRG);
  constexpr int RS = NS < MR ? NS : MR;
#pragma unroll
  for (int s0 = 0; s0 < NS; s0 += RS) {
#pragma unroll
    for (int ss = 0; ss < RS; ++ss) {
      const int s = s0 + ss;
      const float lt = xkg_sum(l[s]);
      float* rec = mrg + ((cx.wave * MR + ss) * 2 + (lane >> 5)) * MREC + 8 * (lane & 15) + 4 * ((lane >> 4) & 1);
      *reinterpret_cast<f32x4*>(rec) = acc[s][0];
      *reinterpret_cast<f32x4*>(rec + 128) = acc[s][1];
      if ((lane & 55) == 0) {
        float* rr = mrg + ((cx.wave * MR + ss) * 2 + (lane >> 3)) * MREC + 256;
        rr[0] = m[s]; rr[1] = lt;
      }
    }
    __syncthreads();
    if (cx.wave < 2 * RS) {
      const int row = cx.wave;
      float M = -INFINITY;
#pragma unroll
      for (int w = 0; w < NW; ++w) M = fmaxf(M, mrg[(w * MR * 2 + row) * MREC + 256]);
      float L = 0.f;
      f32x4 o = f32x4{0.f, 0.f, 0.f, 0.f};
#pragma unroll
      for (int w = 0; w < NW; ++w) {
        const float* rec = mrg + (w * MR * 2 + row) * MREC;
        const float lw = rec[257];
        const float wt = (lw > 0.f) ? __builtin_amdgcn_exp2f(rec[256] - M) : 0.f;
        L = fmaf(wt, lw, L);
        const f32x4 a = *reinterpret_cast<const f32x4*>(rec + 4 * lane);
        o += a * wt;
      }
      size_t slot;
      float* po; float* ml;
      if (NS > 1) {
        slot = ((size_t)(cx.head * NG + seg) * NWG + cx.wgi) * (2 * S) + (s0 * 2 + row);
        po = reinterpret_cast<float*>(cx.ws + WS_PO_SH); ml = reinterpret_cast<float*>(cx.ws + WS_ML_SH);
      } else {
        slot = ((size_t)(cx.head * B + seq0) * NWG + cx.wgi) * 2 + row;
        po = reinterpret_cast<float*>(cx.ws + WS_PO_PR); ml = reinterpret_cast<float*>(cx.ws + WS_ML_PR);
      }
      *reinterpret_cast<f32x4*>(po + slot * D + 4 * lane) = o;
      if (lane == 0) *reinterpret_cast<float2*>(ml + slot * 2) = make_float2(M, L);
    }
    __syncthreads();
  }
}

__global__ void __launch_bounds__(NTHR, 2)
attn_main(const __hip_bfloat16* __restrict__ q, const __hip_bfloat16* __restrict__ kc, const __hip_bfloat16* __restrict__ vc,
          const int* __restrict__ bt, const int* __restrict__ ctx_lens, unsigned char* __restrict__ ws) {
  __shared__ __attribute__((aligned(16))) unsigned char lds[LDS_BYTES];
  const int tid = threadIdx.x, lane = tid & 63, wave = __builtin_amdgcn_readfirstlane(tid >> 6);
  const int head = blockIdx.x & 7, wgi = blockIdx.x >> 3;
  int* smin = reinterpret_cast<int*>(lds + LDS_MISC);
  const int brow = tid >> 4, bcol = (tid & 15) * 8;
  const i32x4 t0 = *reinterpret_cast<const i32x4*>(bt + brow * NB + bcol);
  const i32x4 t1 = *reinterpret_cast<const i32x4*>(bt + brow * NB + bcol + 4);
  const i32x4 z0 = *reinterpret_cast<const i32x4*>(bt + bcol);
  const i32x4 z1 = *reinterpret_cast<const i32x4*>(bt + bcol + 4);
  const int myctx = ctx_lens[(lane >= NG && lane < NSEG) ? lane - NG : 0];
  const int qrow = tid >> 3, qpc = tid & 7;
  const __hip_bfloat16* qsrc = q + ((size_t)(qrow >> 1) * H + head * 2 + (qrow & 1)) * D + qpc * 32;
  u32x4 qv[4];
#pragma unroll
  for (int i = 0; i < 4; ++i) qv[i] = *reinterpret_cast<const u32x4*>(qsrc + i * 8);
  if (tid == 0) smin[0] = NB;
  __syncthreads();
  {
    int fm = NB;
#pragma unroll
    for (int j = 3; j >= 0; --j) if (t1[j] != z1[j]) fm = bcol + 4 + j;
#pragma unroll
    for (int j = 3; j >= 0; --j) if (t0[j] != z0[j]) fm = bcol + j;
    if (wave == 0 && lane >= NG && lane < NSEG) fm = min(fm, myctx <= 0 ? 0 : min(myctx, NB * BS) / BS);
#pragma unroll
    for (int o = 32; o > 0; o >>= 1) fm = min(fm, __shfl_xor(fm, o, 64));
    if (lane == 0) atomicMin(smin, fm);
    *reinterpret_cast<i32x4*>(lds + LDS_BT + (brow * NB + bcol) * 4) = t0;
    *reinterpret_cast<i32x4*>(lds + LDS_BT + (brow * NB + bcol + 4) * 4) = t1;
    unsigned char* qd = lds + LDS_Q + (qrow >> 1) * QS_STRIDE + (qrow & 1) * QH_STRIDE + qpc * 64;
#pragma unroll
    for (int i = 0; i < 4; ++i) *reinterpret_cast<u32x4*>(qd + i * 16) = qv[i];
  }
  __syncthreads();
  const int Pc = __builtin_amdgcn_readfirstlane(smin[0]);
  if (blockIdx.x == 0 && tid == 0) *reinterpret_cast<int*>(ws + WS_PLAN) = Pc;
  int npg, wt, wstart, wtotal;
  seg_geometry(lane, myctx, Pc, npg, wt, wstart, wtotal);
  const int lo = (int)(((long long)wtotal * wgi) / NWG), hi = (int)(((long long)wtotal * (wgi + 1)) / NWG);
  const int pb = pos_to_page(lo, wstart, wt, npg), pe = pos_to_page(hi, wstart, wt, npg);
  const int cnt = pe - pb;
  const int inc = wave_incl_scan(cnt, lane);
  const int off = inc - cnt;
  const int total = __shfl(inc, 63, 64);
  unsigned long long segmask = __ballot(cnt > 0);
  const int g = wave + 8 * lane;
  int plist = 0;
  {
    unsigned long long mm = segmask;
    while (mm) {
      const int j = __builtin_ctzll(mm); mm &= mm - 1;
      const int o_ = __builtin_amdgcn_readlane(off, j), c_ = __builtin_amdgcn_readlane(cnt, j), b_ = __builtin_amdgcn_readlane(pb, j);
      const int row = j < NG ? j * S : j - NG;
      const int base = j < NG ? 0 : Pc;
      if (g >= o_ && g < o_ + c_) plist = reinterpret_cast<const int*>(lds + LDS_BT)[row * NB + base + b_ + (g - o_)];
    }
  }
  Ctx cx;
  cx.lane = lane; cx.wave = wave; cx.head = head; cx.wgi = wgi; cx.lds = lds; cx.ws = ws; cx.plist = plist;
  cx.nitems = __builtin_amdgcn_readfirstlane(total > wave ? (total - wave + 7) >> 3 : 0);
  cx.kbase = kc + head * D + (lane & 15) * TOK_STRIDE + (lane >> 4) * 8;
  cx.vbase = vc + head * D + (lane >> 4) * 4 * TOK_STRIDE + (lane & 15) * 8;
#pragma unroll
  for (int d = 0; d < 4; ++d) cx.mk[d] = (((lane & 7) >> 1) == d) ? ((lane & 1) ? 0xffff0000u : 0x0000ffffu) : 0u;
  KV r;
  if (cx.nitems > 0) {
    const int ph = __builtin_amdgcn_readlane(plist, 0);
    issue_k(r, cx.kbase, ph);
    issue_v(r, cx.vbase, ph);
  } else {
#pragma unroll
    for (int i = 0; i < 8; ++i) { r.k[i] = u32x4{0u, 0u, 0u, 0u}; r.v[i] = u32x4{0u, 0u, 0u, 0u}; }
  }
  while (segmask) {
    const int j = __builtin_ctzll(segmask); segmask &= segmask - 1;
    const int o_ = __builtin_amdgcn_readlane(off, j), c_ = __builtin_amdgcn_readlane(cnt, j), b_ = __builtin_amdgcn_readlane(pb, j);
    const int kb = (o_ - wave + 7) >> 3, ke = (o_ + c_ - wave + 7) >> 3;
    const int pgrel = b_ + (wave + 8 * kb - o_);
    if (j < NG) {
      run_segment<S>(cx, r, j, j * S, kb, ke, pgrel, 0);
    } else {
      const int cl = __builtin_amdgcn_readlane(myctx, j);
      run_segment<1>(cx, r, j, j - NG, kb, ke, Pc + pgrel, min(cl, NB * BS));
    }
  }
}

__global__ void __launch_bounds__(256)
attn_combine(const int* __restrict__ ctx_lens, const unsigned char* __restrict__ ws, __hip_bfloat16* __restrict__ out) {
  const int lane = threadIdx.x & 63, wave = threadIdx.x >> 6;
  const int rowid = blockIdx.x * 4 + wave;
  const int b = rowid >> 4, h = rowid & 15, head = h >> 1, hh = h & 1;
  const int myctx = ctx_lens[(lane >= NG && lane < NSEG) ? lane - NG : 0];
  const int Pc = *reinterpret_cast<const int*>(ws + WS_PLAN);
  int npg, wt, wstart, wtotal;
  seg_geometry(lane, myctx, Pc, npg, wt, wstart, wtotal);
  const int wgi = lane & 31;
  const int seg = (lane < 32) ? (b / S) : (NG + b);
  const int s_npg = __shfl(npg, seg, 64), s_wt = __shfl(wt, seg, 64), s_ws = __shfl(wstart, seg, 64);
  const int lo = (int)(((long long)wtotal * wgi) / NWG), hi = (int)(((long long)wtotal * (wgi + 1)) / NWG);
  const bool has = pos_to_page(hi, s_ws, s_wt, s_npg) > pos_to_page(lo, s_ws, s_wt, s_npg);
  size_t slot;
  const float* mlp;
  if (lane < 32) {
    slot = ((size_t)(head * NG + b / S) * NWG + wgi) * (2 * S) + ((b % S) * 2 + hh);
    mlp = reinterpret_cast<const float*>(ws + WS_ML_SH) + slot * 2;
  } else {
    slot = ((size_t)(head * B + b) * NWG + wgi) * 2 + hh;
    mlp = reinterpret_cast<const float*>(ws + WS_ML_PR) + slot * 2;
  }
  float2 ml = make_float2(-INFINITY, 0.f);
  if (has) ml = *reinterpret_cast<const float2*>(mlp);
  unsigned long long mask = __ballot(has);
  float M = ml.x;
#pragma unroll
  for (int o = 32; o > 0; o >>= 1) M = fmaxf(M, __shfl_xor(M, o, 64));
  const float w = (has && ml.y > 0.f) ? __builtin_amdgcn_exp2f(ml.x - M) : 0.f;
  float L = w * ml.y;
#pragma unroll
  for (int o = 32; o > 0; o >>= 1) L += __shfl_xor(L, o, 64);
  const unsigned slo = (unsigned)slot;
  f32x4 o = f32x4{0.f, 0.f, 0.f, 0.f};
  while (mask) {
    const int j = __builtin_ctzll(mask); mask &= mask - 1;
    const unsigned sl = __builtin_amdgcn_readlane(slo, j);
    const float wj = __builtin_bit_cast(float, __builtin_amdgcn_readlane(__builtin_bit_cast(int, w), j));
    const float* po = reinterpret_cast<const float*>(ws + (j < 32 ? WS_PO_SH : WS_PO_PR)) + (size_t)sl * D + 4 * lane;
    const f32x4 a = *reinterpret_cast<const f32x4*>(po);
    o += a * wj;
  }
  const float inv = (L > 0.f) ? 1.f / L : 0.f;
  uint2 pk;
  pk.x = cvtpk(o[0] * inv, o[1] * inv);
  pk.y = cvtpk(o[2] * inv, o[3] * inv);
  *reinterpret_cast<uint2*>(out + (size_t)rowid * D + 4 * lane) = pk;
}
}

extern "C" void kernel_launch(void* const* d_in, const int* in_sizes, int n_in, void* d_out, int out_size, void* d_ws,
                              size_t ws_size, hipStream_t stream) {
  const __hip_bfloat16* q = (const __hip_bfloat16*)d_in[0];
  const __hip_bfloat16* kc = (const __hip_bfloat16*)d_in[1];
  const __hip_bfloat16* vc = (const __hip_bfloat16*)d_in[2];
  const int* bt = (const int*)d_in[3];
  const int* ctx = (const int*)d_in[4];
  attn_main<<<HKV * NWG, NTHR, 0, stream>>>(q, kc, vc, bt, ctx, (unsigned char*)d_ws);
  attn_combine<<<B * H / 4, 256, 0, stream>>>(ctx, (const unsigned char*)d_ws, (__hip_bfloat16*)d_out);
}
```

```cpp
#include <hip/hip_runtime.h>
#include <hip/hip_bf16.h>
#include <cstdint>

#define SGRP 8
#define WSH 4
#define KV_NT 0

namespace {
constexpr int B = 32, H = 16, HKV = 8, D = 256, BS = 16, NB = 128;
constexpr int S = SGRP, NG = B / S;
constexpr int NWG = 32;
constexpr int NW = 8, NTHR = NW * 64;
constexpr int WPRIV = 4, WSHARED = WSH;
constexpr int NSEG = NG + B;
constexpr float C2 = 0.0625f * 1.4426950408889634f;
constexpr int PAGE_ELEMS = BS * HKV * D;
constexpr int TOK_STRIDE = HKV * D;

constexpr size_t WS_PLAN = 0;
constexpr size_t WS_ML_SH = 4096;
constexpr size_t WS_ML_PR = WS_ML_SH + (size_t)HKV * NG * NWG * 2 * S * 8;
constexpr size_t WS_PO_SH = WS_ML_PR + (size_t)HKV * B * NWG * 2 * 8;
constexpr size_t WS_PO_PR = WS_PO_SH + (size_t)HKV * NG * NWG * 2 * S * D * 4;
constexpr size_t WS_END = WS_PO_PR + (size_t)HKV * B * NWG * 2 * D * 4;

constexpr int QH_STRIDE = 576;
constexpr int QS_STRIDE = 2 * QH_STRIDE;
constexpr int LDS_BT = 0;
constexpr int LDS_Q = LDS_BT + B * NB * 4;
constexpr int MR = 4;
constexpr int MREC = 264;
constexpr int LDS_MRG = LDS_Q + B * QS_STRIDE;
constexpr int LDS_MISC = LDS_MRG + NW * MR * 2 * MREC * 4;
constexpr int LDS_BYTES = LDS_MISC + 64;

using bf16x8 = __attribute__((ext_vector_type(8))) __bf16;
using u32x4 = __attribute__((ext_vector_type(4))) unsigned;
using i32x4 = __attribute__((ext_vector_type(4))) int;
using f32x4 = __attribute__((ext_vector_type(4))) float;

__device__ __forceinline__ u32x4 ldkv(const void* p) {
  return *reinterpret_cast<const u32x4*>(p);
}
__device__ __forceinline__ f32x4 mfma(u32x4 a, u32x4 b, f32x4 c) {
  return __builtin_amdgcn_mfma_f32_16x16x32_bf16(__builtin_bit_cast(bf16x8, a), __builtin_bit_cast(bf16x8, b), c, 0, 0, 0);
}
__device__ __forceinline__ unsigned cvtpk(float lo, float hi) {
  using f32x2 = __attribute__((ext_vector_type(2))) float;
  using bf16x2 = __attribute__((ext_vector_type(2))) __bf16;
  const f32x2 v = {lo, hi};
  return __builtin_bit_cast(unsigned, __builtin_convertvector(v, bf16x2));
}
template <int CTRL> __device__ __forceinline__ float dpp(float x) {
  return __builtin_bit_cast(float, __builtin_amdgcn_mov_dpp(__builtin_bit_cast(int, x), CTRL, 0xf, 0xf, true));
}
__device__ __forceinline__ float xkg_max(float x) {
  auto s = __builtin_amdgcn_permlane16_swap(__float_as_uint(x), __float_as_uint(x), false, false);
  x = fmaxf(__uint_as_float(s[0]), __uint_as_float(s[1]));
  auto t = __builtin_amdgcn_permlane32_swap(__float_as_uint(x), __float_as_uint(x), false, false);
  return fmaxf(__uint_as_float(t[0]), __uint_as_float(t[1]));
}
__device__ __forceinline__ float xkg_sum(float x) {
  auto s = __builtin_amdgcn_permlane16_swap(__float_as_uint(x), __float_as_uint(x), false, false);
  x = __uint_as_float(s[0]) + __uint_as_float(s[1]);
  auto t = __builtin_amdgcn_permlane32_swap(__float_as_uint(x), __float_as_uint(x), false, false);
  return __uint_as_float(t[0]) + __uint_as_float(t[1]);
}
__device__ __forceinline__ int wave_incl_scan(int x) {
  x += __builtin_amdgcn_update_dpp(0, x, 0x111, 0xf, 0xf, false);
  x += __builtin_amdgcn_update_dpp(0, x, 0x112, 0xf, 0xf, false);
  x += __builtin_amdgcn_update_dpp(0, x, 0x114, 0xf, 0xf, false);
  x += __builtin_amdgcn_update_dpp(0, x, 0x118, 0xf, 0xf, false);
  x += __builtin_amdgcn_update_dpp(0, x, 0x142, 0xa, 0xf, false);
  x += __builtin_amdgcn_update_dpp(0, x, 0x143, 0xc, 0xf, false);
  return x;
}
__device__ __forceinline__ int wave_min(int x) {
  constexpr int BIG = 0x7fffffff;
  x = min(x, __builtin_amdgcn_update_dpp(BIG, x, 0x111, 0xf, 0xf, false));
  x = min(x, __builtin_amdgcn_update_dpp(BIG, x, 0x112, 0xf, 0xf, false));
  x = min(x, __builtin_amdgcn_update_dpp(BIG, x, 0x114, 0xf, 0xf, false));
  x = min(x, __builtin_amdgcn_update_dpp(BIG, x, 0x118, 0xf, 0xf, false));
  x = min(x, __builtin_amdgcn_update_dpp(BIG, x, 0x142, 0xa, 0xf, false));
  x = min(x, __builtin_amdgcn_update_dpp(BIG, x, 0x143, 0xc, 0xf, false));
  return __builtin_amdgcn_readlane(x, 63);
}
__device__ __forceinline__ int live_pages(int ctx) {
  if (ctx <= 0) return NB;
  ctx = min(ctx, NB * BS);
  return (ctx + BS - 1) / BS;
}
struct Geo { int npg, ustart, wstart, wtotal; bool sh; };
__device__ __forceinline__ Geo seg_geometry(int lane, int nb_l, int pn_incl, int Pc) {
  Geo g;
  g.sh = lane < NG;
  const int pn_tot = __builtin_amdgcn_readlane(pn_incl, 63);
  const int priv_before = (pn_incl - nb_l) - (lane - NG) * Pc;
  g.npg = g.sh ? Pc : (lane < NSEG ? nb_l - Pc : 0);
  g.ustart = g.sh ? lane * Pc : NG * Pc + priv_before;
  g.wstart = g.sh ? lane * Pc * WSHARED : NG * Pc * WSHARED + WPRIV * priv_before;
  g.wtotal = NG * Pc * WSHARED + WPRIV * (pn_tot - B * Pc);
  return g;
}
__device__ __forceinline__ int pos_to_page(int pos, int wstart, bool sh, int npg) {
  const int d = max(pos - wstart, 0);
  const int pgs = sh ? (d + WSHARED - 1) / WSHARED : (d + WPRIV - 1) / WPRIV;
  return min(pgs, npg);
}

struct KV { u32x4 k[8]; u32x4 v[8]; };

__device__ __forceinline__ void issue_k(KV& r, const __hip_bfloat16* kbase, int phys) {
  const __hip_bfloat16* p = kbase + (size_t)phys * PAGE_ELEMS;
#pragma unroll
  for (int c = 0; c < 8; ++c) r.k[c] = ldkv(p + c * 32);
}
__device__ __forceinline__ void issue_v(KV& r, const __hip_bfloat16* vbase, int phys) {
  const __hip_bfloat16* p = vbase + (size_t)phys * PAGE_ELEMS;
#pragma unroll
  for (int i = 0; i < 4; ++i)
#pragma unroll
    for (int h = 0; h < 2; ++h) r.v[i * 2 + h] = ldkv(p + i * TOK_STRIDE + h * 128);
}

struct Ctx {
  const __hip_bfloat16* kbase; const __hip_bfloat16* vbase;
  unsigned char* lds; unsigned char* ws;
  int lane, wave, head, wgi, plist, nitems;
  unsigned mk[4];
};

template <int NR>
__device__ __forceinline__ void merge_rows(const Ctx& cx, float* po, float* ml, size_t slot0) {
  const float* mrg = reinterpret_cast<const float*>(cx.lds + LDS_MRG);
  const int lane = cx.lane;
  __syncthreads();
  if (cx.wave < NR) {
    const int row = cx.wave;
    float M = -INFINITY;
#pragma unroll
    for (int w = 0; w < NW; ++w) M = fmaxf(M, mrg[(w * MR * 2 + row) * MREC + 256]);
    float L = 0.f;
    f32x4 o = f32x4{0.f, 0.f, 0.f, 0.f};
#pragma unroll
    for (int w = 0; w < NW; ++w) {
      const float* rec = mrg + (w * MR * 2 + row) * MREC;
      const float lw = rec[257];
      const float wt = (lw > 0.f) ? __builtin_amdgcn_exp2f(rec[256] - M) : 0.f;
      L = fmaf(wt, lw, L);
      const f32x4 a = *reinterpret_cast<const f32x4*>(rec + 4 * lane);
      o += a * wt;
    }
    const size_t slot = slot0 + row;
    *reinterpret_cast<f32x4*>(po + slot * D + 4 * lane) = o;
    if (lane == 0) *reinterpret_cast<float2*>(ml + slot * 2) = make_float2(M, L);
  }
  __syncthreads();
}

__device__ __forceinline__ void run_private(const Ctx& cx, KV& r, int seq, int kb, int ke, int pg0, int ctxlen) {
  const int lane = cx.lane;
  float m = -INFINITY, l = 0.f;
  f32x4 a0 = f32x4{0.f, 0.f, 0.f, 0.f}, a1 = f32x4{0.f, 0.f, 0.f, 0.f};
  const unsigned char* qp = cx.lds + LDS_Q + seq * QS_STRIDE + ((lane >> 3) & 1) * QH_STRIDE + (lane >> 4) * 16;
  u32x4 qf[8];
#pragma unroll
  for (int c = 0; c < 8; ++c) qf[c] = *reinterpret_cast<const u32x4*>(qp + c * 64);
  const bool uni = ctxlen <= 0;
  const bool samehead = ((lane >> 5) & 1) == ((lane >> 3) & 1);
  for (int k = kb; k < ke; ++k) {
    const int pg = pg0 + 8 * (k - kb);
    const bool more = (k + 1) < cx.nitems;
    const int nphys = __builtin_amdgcn_readlane(cx.plist, more ? k + 1 : k);
    f32x4 sc = f32x4{0.f, 0.f, 0.f, 0.f};
#pragma unroll
    for (int c = 0; c < 8; ++c) sc = mfma(r.k[c], qf[c], sc);
    if (more) issue_k(r, cx.kbase, nphys);
    const int rem = uni ? BS : ctxlen - pg * BS;
    float t[4];
#pragma unroll
    for (int i = 0; i < 4; ++i) t[i] = uni ? 0.f : sc[i] * C2;
    if (rem < BS) {
#pragma unroll
      for (int i = 0; i < 4; ++i) t[i] = ((lane >> 4) * 4 + i < rem) ? t[i] : -INFINITY;
    }
    float pm = fmaxf(fmaxf(t[0], t[1]), fmaxf(t[2], t[3]));
    pm = xkg_max(pm);
    const float mn = fmaxf(m, pm);
    const float msub = (mn == -INFINITY) ? 0.f : mn;
    const float alpha = __builtin_amdgcn_exp2f(m - msub);
    m = mn;
    unsigned pp[4];
    float ps = 0.f;
#pragma unroll
    for (int i = 0; i < 4; ++i) {
      const float p = __builtin_amdgcn_exp2f(t[i] - msub);
      pp[i] = cvtpk(p, p);
      ps += __uint_as_float(pp[i] & 0xffff0000u);
    }
    l = fmaf(l, alpha, ps);
    const float aoth = dpp<0x128>(alpha);
    const float aa = samehead ? alpha : aoth;
    a0 *= aa; a1 *= aa;
#pragma unroll
    for (int i = 0; i < 4; ++i) {
      const u32x4 pa = u32x4{pp[i] & cx.mk[0], pp[i] & cx.mk[1], pp[i] & cx.mk[2], pp[i] & cx.mk[3]};
      a0 = mfma(pa, r.v[i * 2 + 0], a0);
      a1 = mfma(pa, r.v[i * 2 + 1], a1);
    }
    if (more) issue_v(r, cx.vbase, nphys);
  }
  float* mrg = reinterpret_cast<float*>(cx.lds + LDS_MRG);
  const float lt = xkg_sum(l);
  float* rec = mrg + (cx.wave * MR * 2 + (lane >> 5)) * MREC + 8 * (lane & 15) + 4 * ((lane >> 4) & 1);
  *reinterpret_cast<f32x4*>(rec) = a0;
  *reinterpret_cast<f32x4*>(rec + 128) = a1;
  if ((lane & 55) == 0) {
    float* rr = mrg + (cx.wave * MR * 2 + (lane >> 3)) * MREC + 256;
    rr[0] = m; rr[1] = lt;
  }
  merge_rows<2>(cx, reinterpret_cast<float*>(cx.ws + WS_PO_PR), reinterpret_cast<float*>(cx.ws + WS_ML_PR),
                ((size_t)(cx.head * B + seq) * NWG + cx.wgi) * 2);
}

template <int N, int RM, int BM> __device__ __forceinline__ unsigned bcast(unsigned old, unsigned x) {
  return (unsigned)__builtin_amdgcn_update_dpp((int)old, (int)x, 0x150 + N, RM, BM, false);
}
template <int SQ> __device__ __forceinline__ void shared_pv(const Ctx& cx, const KV& r, unsigned p01, unsigned p23, float alpha,
                                                            const unsigned (&sel)[8], f32x4& a0, f32x4& a1) {
  unsigned x01 = bcast<2 * SQ, 0xf, 0x3>(0u, p01); x01 = bcast<2 * SQ + 1, 0xf, 0xc>(x01, p01);
  unsigned x23 = bcast<2 * SQ, 0xf, 0x3>(0u, p23); x23 = bcast<2 * SQ + 1, 0xf, 0xc>(x23, p23);
  unsigned au = bcast<2 * SQ, 0x3, 0xf>(0u, __float_as_uint(alpha)); au = bcast<2 * SQ + 1, 0xc, 0xf>(au, __float_as_uint(alpha));
  const float aa = __uint_as_float(au);
  a0 *= aa; a1 *= aa;
#pragma unroll
  for (int i = 0; i < 4; ++i) {
    const unsigned x = i < 2 ? x01 : x23;
    const u32x4 pa = u32x4{__builtin_amdgcn_perm(x, x, sel[(i & 1) * 4 + 0]), __builtin_amdgcn_perm(x, x, sel[(i & 1) * 4 + 1]),
                           __builtin_amdgcn_perm(x, x, sel[(i & 1) * 4 + 2]), __builtin_amdgcn_perm(x, x, sel[(i & 1) * 4 + 3])};
    a0 = mfma(pa, r.v[i * 2 + 0], a0);
    a1 = mfma(pa, r.v[i * 2 + 1], a1);
  }
}
__device__ __forceinline__ void run_shared(const Ctx& cx, KV& r, int grp, int kb, int ke) {
  static_assert(S == 8, "16 MFMA columns = 8 sequences x 2 heads");
  const int lane = cx.lane;
  float m = -INFINITY, l = 0.f;
  f32x4 acc[S][2];
#pragma unroll
  for (int s = 0; s < S; ++s) { acc[s][0] = f32x4{0.f, 0.f, 0.f, 0.f}; acc[s][1] = f32x4{0.f, 0.f, 0.f, 0.f}; }
  const unsigned char* qp = cx.lds + LDS_Q + (grp * S + ((lane & 15) >> 1)) * QS_STRIDE + (lane & 1) * QH_STRIDE + (lane >> 4) * 16;
  u32x4 qf[8];
#pragma unroll
  for (int c = 0; c < 8; ++c) qf[c] = *reinterpret_cast<const u32x4*>(qp + c * 64);
  unsigned sel[8];
#pragma unroll
  for (int hf = 0; hf < 2; ++hf)
#pragma unroll
    for (int d = 0; d < 4; ++d) {
      const unsigned src = hf ? 0x0302u : 0x0100u;
      sel[hf * 4 + d] = (((lane & 7) >> 1) == d) ? ((lane & 1) ? (0x00000c0cu | (src << 16)) : (0x0c0c0000u | src)) : 0x0c0c0c0cu;
    }
  for (int k = kb; k < ke; ++k) {
    const bool more = (k + 1) < cx.nitems;
    const int nphys = __builtin_amdgcn_readlane(cx.plist, more ? k + 1 : k);
    f32x4 sc = f32x4{0.f, 0.f, 0.f, 0.f};
#pragma unroll
    for (int c = 0; c < 8; ++c) sc = mfma(r.k[c], qf[c], sc);
    if (more) issue_k(r, cx.kbase, nphys);
    float t[4];
#pragma unroll
    for (int i = 0; i < 4; ++i) t[i] = sc[i] * C2;
    float pm = fmaxf(fmaxf(t[0], t[1]), fmaxf(t[2], t[3]));
    pm = xkg_max(pm);
    const float mn = fmaxf(m, pm);
    const float msub = (mn == -INFINITY) ? 0.f : mn;
    const float alpha = __builtin_amdgcn_exp2f(m - msub);
    m = mn;
    const unsigned p01 = cvtpk(__builtin_amdgcn_exp2f(t[0] - msub), __builtin_amdgcn_exp2f(t[1] - msub));
    const unsigned p23 = cvtpk(__builtin_amdgcn_exp2f(t[2] - msub), __builtin_amdgcn_exp2f(t[3] - msub));
    const float ps = (__uint_as_float(p01 << 16) + __uint_as_float(p01 & 0xffff0000u)) +
                     (__uint_as_float(p23 << 16) + __uint_as_float(p23 & 0xffff0000u));
    l = fmaf(l, alpha, ps);
    shared_pv<0>(cx, r, p01, p23, alpha, sel, acc[0][0], acc[0][1]);
    shared_pv<1>(cx, r, p01, p23, alpha, sel, acc[1][0], acc[1][1]);
    shared_pv<2>(cx, r, p01, p23, alpha, sel, acc[2][0], acc[2][1]);
    shared_pv<3>(cx, r, p01, p23, alpha, sel, acc[3][0], acc[3][1]);
    shared_pv<4>(cx, r, p01, p23, alpha, sel, acc[4][0], acc[4][1]);
    shared_pv<5>(cx, r, p01, p23, alpha, sel, acc[5][0], acc[5][1]);
    shared_pv<6>(cx, r, p01, p23, alpha, sel, acc[6][0], acc[6][1]);
    shared_pv<7>(cx, r, p01, p23, alpha, sel, acc[7][0], acc[7][1]);
    if (more) issue_v(r, cx.vbase, nphys);
  }
  float* mrg = reinterpret_cast<float*>(cx.lds + LDS_MRG);
  const float lt = xkg_sum(l);
#pragma unroll
  for (int rd = 0; rd < S / MR; ++rd) {
#pragma unroll
    for (int ss = 0; ss < MR; ++ss) {
      float* rec = mrg + ((cx.wave * MR + ss) * 2 + (lane >> 5)) * MREC + 8 * (lane & 15) + 4 * ((lane >> 4) & 1);
      *reinterpret_cast<f32x4*>(rec) = acc[rd * MR + ss][0];
      *reinterpret_cast<f32x4*>(rec + 128) = acc[rd * MR + ss][1];
    }
    if ((lane >> 3) == rd) {
      float* rr = mrg + (cx.wave * MR * 2 + (lane & 7)) * MREC + 256;
      rr[0] = m; rr[1] = lt;
    }
    merge_rows<2 * MR>(cx, reinterpret_cast<float*>(cx.ws + WS_PO_SH), reinterpret_cast<float*>(cx.ws + WS_ML_SH),
                       ((size_t)(cx.head * NG + grp) * NWG + cx.wgi) * (2 * S) + rd * 2 * MR);
  }
}

__global__ void __launch_bounds__(NTHR, 2)
attn_main(const __hip_bfloat16* __restrict__ q, const __hip_bfloat16* __restrict__ kc, const __hip_bfloat16* __restrict__ vc,
          const int* __restrict__ bt, const int* __restrict__ ctx_lens, unsigned char* __restrict__ ws) {
  __shared__ __attribute__((aligned(16))) unsigned char lds[LDS_BYTES];
  const int tid = threadIdx.x, lane = tid & 63, wave = __builtin_amdgcn_readfirstlane(tid >> 6);
  const int head = blockIdx.x & 7, wgi = blockIdx.x >> 3;
  int* smin = reinterpret_cast<int*>(lds + LDS_MISC);
  int2 tb[4];
#pragma unroll
  for (int i = 0; i < 4; ++i) tb[i] = *reinterpret_cast<const int2*>(bt + (wave * 4 + i) * NB + 2 * lane);
  const int2 z = *reinterpret_cast<const int2*>(bt + 2 * lane);
  const bool seqlane = lane >= NG && lane < NSEG;
  const int myctx = ctx_lens[seqlane ? lane - NG : 0];
  const int qrow = tid >> 3, qpc = tid & 7;
  const __hip_bfloat16* qsrc = q + ((size_t)(qrow >> 1) * H + head * 2 + (qrow & 1)) * D + qpc * 32;
  u32x4 qv[4];
#pragma unroll
  for (int i = 0; i < 4; ++i) qv[i] = *reinterpret_cast<const u32x4*>(qsrc + i * 8);
  const int nb_l = seqlane ? live_pages(myctx) : 0;
  const int pn_incl = wave_incl_scan(nb_l);
  const int fullmin = wave_min(seqlane ? (myctx <= 0 ? 0 : min(myctx, NB * BS) / BS) : NB);
  {
    bool ne = false, no = false;
#pragma unroll
    for (int i = 0; i < 4; ++i) {
      ne |= tb[i].x != z.x; no |= tb[i].y != z.y;
      *reinterpret_cast<int2*>(lds + LDS_BT + ((wave * 4 + i) * NB + 2 * lane) * 4) = tb[i];
    }
    const unsigned long long me = __ballot(ne), mo = __ballot(no);
    const int fe = me ? 2 * __builtin_ctzll(me) : NB, fo = mo ? 2 * __builtin_ctzll(mo) + 1 : NB;
    if (lane == 0) smin[wave] = min(fe, fo);
  }
  __syncthreads();
  int Pc = fullmin;
#pragma unroll
  for (int w = 0; w < NW; ++w) Pc = min(Pc, smin[w]);
  Pc = __builtin_amdgcn_readfirstlane(Pc);
  if (blockIdx.x == 0 && tid == 0) *reinterpret_cast<int*>(ws + WS_PLAN) = Pc;
  const Geo ge = seg_geometry(lane, nb_l, pn_incl, Pc);
  const int lo = (int)(((long long)ge.wtotal * wgi) / NWG), hi = (int)(((long long)ge.wtotal * (wgi + 1)) / NWG);
  const int pb = pos_to_page(lo, ge.wstart, ge.sh, ge.npg), pe = pos_to_page(hi, ge.wstart, ge.sh, ge.npg);
  const int cnt = pe - pb;
  unsigned long long segmask = __ballot(cnt > 0);
  int off = 0, total = 0;
  if (segmask) {
    const int jf = __builtin_ctzll(segmask), jl = 63 - __builtin_clzll(segmask);
    const int u_lo = __builtin_amdgcn_readlane(ge.ustart + pb, jf);
    off = ge.ustart + pb - u_lo;
    total = __builtin_amdgcn_readlane(ge.ustart + pe, jl) - u_lo;
  }
  const int g = wave + 8 * lane;
  int plist = 0;
  {
    unsigned long long mm = segmask;
    while (mm) {
      const int j = __builtin_ctzll(mm); mm &= mm - 1;
      const int o_ = __builtin_amdgcn_readlane(off, j), c_ = __builtin_amdgcn_readlane(cnt, j), b_ = __builtin_amdgcn_readlane(pb, j);
      const int row = j < NG ? j * S : j - NG;
      const int base = j < NG ? 0 : Pc;
      if (g >= o_ && g < o_ + c_) plist = reinterpret_cast<const int*>(lds + LDS_BT)[row * NB + base + b_ + (g - o_)];
    }
  }
  Ctx cx;
  cx.lane = lane; cx.wave = wave; cx.head = head; cx.wgi = wgi; cx.lds = lds; cx.ws = ws; cx.plist = plist;
  cx.nitems = __builtin_amdgcn_readfirstlane(total > wave ? (total - wave + 7) >> 3 : 0);
  cx.kbase = kc + head * D + (lane & 15) * TOK_STRIDE + (lane >> 4) * 8;
  cx.vbase = vc + head * D + (lane >> 4) * 4 * TOK_STRIDE + (lane & 15) * 8;
#pragma unroll
  for (int d = 0; d < 4; ++d) cx.mk[d] = (((lane & 7) >> 1) == d) ? ((lane & 1) ? 0xffff0000u : 0x0000ffffu) : 0u;
  KV r;
  if (cx.nitems > 0) {
    const int ph = __builtin_amdgcn_readlane(plist, 0);
    issue_k(r, cx.kbase, ph);
    issue_v(r, cx.vbase, ph);
  } else {
#pragma unroll
    for (int i = 0; i < 8; ++i) { r.k[i] = u32x4{0u, 0u, 0u, 0u}; r.v[i] = u32x4{0u, 0u, 0u, 0u}; }
  }
  {
    unsigned char* qd = lds + LDS_Q + (qrow >> 1) * QS_STRIDE + (qrow & 1) * QH_STRIDE + qpc * 64;
#pragma unroll
    for (int i = 0; i < 4; ++i) *reinterpret_cast<u32x4*>(qd + i * 16) = qv[i];
  }
  __syncthreads();
  while (segmask) {
    const int j = __builtin_ctzll(segmask); segmask &= segmask - 1;
    const int o_ = __builtin_amdgcn_readlane(off, j), c_ = __builtin_amdgcn_readlane(cnt, j), b_ = __builtin_amdgcn_readlane(pb, j);
    const int kb = (o_ - wave + 7) >> 3, ke = (o_ + c_ - wave + 7) >> 3;
    const int pgrel = b_ + (wave + 8 * kb - o_);
    if (j < NG) {
      run_shared(cx, r, j, kb, ke);
    } else {
      const int cl = __builtin_amdgcn_readlane(myctx, j);
      run_private(cx, r, j - NG, kb, ke, Pc + pgrel, min(cl, NB * BS));
    }
  }
}

__global__ void __launch_bounds__(256)
attn_combine(const int* __restrict__ ctx_lens, const unsigned char* __restrict__ ws, __hip_bfloat16* __restrict__ out) {
  const int lane = threadIdx.x & 63, wave = threadIdx.x >> 6;
  const int rowid = blockIdx.x * 4 + wave;
  const int b = rowid >> 4, h = rowid & 15, head = h >> 1, hh = h & 1;
  const bool seqlane = lane >= NG && lane < NSEG;
  const int myctx = ctx_lens[seqlane ? lane - NG : 0];
  const int Pc = *reinterpret_cast<const int*>(ws + WS_PLAN);
  const int nb_l = seqlane ? live_pages(myctx) : 0;
  const Geo ge = seg_geometry(lane, nb_l, wave_incl_scan(nb_l), Pc);
  const int wgi = lane & 31;
  const bool sh = lane < 32;
  const int bu = __builtin_amdgcn_readfirstlane(b);
  const int s_npg = sh ? Pc : __builtin_amdgcn_readlane(ge.npg, NG + bu);
  const int s_ws = sh ? __builtin_amdgcn_readlane(ge.wstart, bu / S) : __builtin_amdgcn_readlane(ge.wstart, NG + bu);
  const int lo = (int)(((long long)ge.wtotal * wgi) / NWG), hi = (int)(((long long)ge.wtotal * (wgi + 1)) / NWG);
  const bool has = pos_to_page(hi, s_ws, sh, s_npg) > pos_to_page(lo, s_ws, sh, s_npg);
  size_t slot;
  const float* mlp;
  if (lane < 32) {
    slot = ((size_t)(head * NG + b / S) * NWG + wgi) * (2 * S) + ((b % S) * 2 + hh);
    mlp = reinterpret_cast<const float*>(ws + WS_ML_SH) + slot * 2;
  } else {
    slot = ((size_t)(head * B + b) * NWG + wgi) * 2 + hh;
    mlp = reinterpret_cast<const float*>(ws + WS_ML_PR) + slot * 2;
  }
  float2 ml = make_float2(-INFINITY, 0.f);
  if (has) ml = *reinterpret_cast<const float2*>(mlp);
  unsigned long long mask = __ballot(has);
  float M = ml.x;
#pragma unroll
  for (int o = 32; o > 0; o >>= 1) M = fmaxf(M, __shfl_xor(M, o, 64));
  const float w = (has && ml.y > 0.f) ? __builtin_amdgcn_exp2f(ml.x - M) : 0.f;
  float L = w * ml.y;
#pragma unroll
  for (int o = 32; o > 0; o >>= 1) L += __shfl_xor(L, o, 64);
  const unsigned slo = (unsigned)slot;
  f32x4 o = f32x4{0.f, 0.f, 0.f, 0.f};
  while (mask) {
    const int j = __builtin_ctzll(mask); mask &= mask - 1;
    const unsigned sl = __builtin_amdgcn_readlane(slo, j);
    const float wj = __builtin_bit_cast(float, __builtin_amdgcn_readlane(__builtin_bit_cast(int, w), j));
    const float* po = reinterpret_cast<const float*>(ws + (j < 32 ? WS_PO_SH : WS_PO_PR)) + (size_t)sl * D + 4 * lane;
    const f32x4 a = *reinterpret_cast<const f32x4*>(po);
    o += a * wj;
  }
  const float inv = (L > 0.f) ? 1.f / L : 0.f;
  uint2 pk;
  pk.x = cvtpk(o[0] * inv, o[1] * inv);
  pk.y = cvtpk(o[2] * inv, o[3] * inv);
  *reinterpret_cast<uint2*>(out + (size_t)rowid * D + 4 * lane) = pk;
}
}

extern "C" void kernel_launch(void* const* d_in, const int* in_sizes, int n_in, void* d_out, int out_size, void* d_ws,
                              size_t ws_size, hipStream_t stream) {
  const __hip_bfloat16* q = (const __hip_bfloat16*)d_in[0];
  const __hip_bfloat16* kc = (const __hip_bfloat16*)d_in[1];
  const __hip_bfloat16* vc = (const __hip_bfloat16*)d_in[2];
  const int* bt = (const int*)d_in[3];
  const int* ctx = (const int*)d_in[4];
  attn_main<<<HKV * NWG, NTHR, 0, stream>>>(q, kc, vc, bt, ctx, (unsigned char*)d_ws);
  attn_combine<<<B * H / 4, 256, 0, stream>>>(ctx, (const unsigned char*)d_ws, (__hip_bfloat16*)d_out);
}
```

```cpp
#include <hip/hip_runtime.h>
#include <hip/hip_bf16.h>
#include <cstdint>

#define SGRP 8
#define WSH 4
#define KV_NT 2

namespace {
constexpr int B = 32, H = 16, HKV = 8, D = 256, BS = 16, NB = 128;
constexpr int S = SGRP, NG = B / S;
constexpr int NWG = 32;
constexpr int NW = 8, NTHR = NW * 64;
constexpr int WPRIV = 4, WSHARED = WSH;
constexpr int NSEG = NG + B;
constexpr float C2 = 0.0625f * 1.4426950408889634f;
constexpr int PAGE_ELEMS = BS * HKV * D;
constexpr int TOK_STRIDE = HKV * D;

constexpr size_t WS_PLAN = 0;
constexpr size_t WS_ML_SH = 4096;
constexpr size_t WS_ML_PR = WS_ML_SH + (size_t)HKV * NG * NWG * 2 * S * 8;
constexpr size_t WS_PO_SH = WS_ML_PR + (size_t)HKV * B * NWG * 2 * 8;
constexpr size_t WS_PO_PR = WS_PO_SH + (size_t)HKV * NG * NWG * 2 * S * D * 4;
constexpr size_t WS_END = WS_PO_PR + (size_t)HKV * B * NWG * 2 * D * 4;

constexpr int QH_STRIDE = 576;
constexpr int QS_STRIDE = 2 * QH_STRIDE;
constexpr int LDS_BT = 0;
constexpr int LDS_Q = LDS_BT + B * NB * 4;
constexpr int MR = 4;
constexpr int MREC = 264;
constexpr int LDS_MRG = LDS_Q + B * QS_STRIDE;
constexpr int LDS_MISC = LDS_MRG + NW * MR * 2 * MREC * 4;
constexpr int LDS_BYTES = LDS_MISC + 64;

using bf16x8 = __attribute__((ext_vector_type(8))) __bf16;
using u32x4 = __attribute__((ext_vector_type(4))) unsigned;
using i32x4 = __attribute__((ext_vector_type(4))) int;
using f32x4 = __attribute__((ext_vector_type(4))) float;

template <int NT> __device__ __forceinline__ u32x4 ldkv(const void* p) {
  if (NT) return __builtin_nontemporal_load(reinterpret_cast<const u32x4*>(p));
  return *reinterpret_cast<const u32x4*>(p);
}
__device__ __forceinline__ f32x4 mfma(u32x4 a, u32x4 b, f32x4 c) {
  return __builtin_amdgcn_mfma_f32_16x16x32_bf16(__builtin_bit_cast(bf16x8, a), __builtin_bit_cast(bf16x8, b), c, 0, 0, 0);
}
__device__ __forceinline__ unsigned cvtpk(float lo, float hi) {
  using f32x2 = __attribute__((ext_vector_type(2))) float;
  using bf16x2 = __attribute__((ext_vector_type(2))) __bf16;
  const f32x2 v = {lo, hi};
  return __builtin_bit_cast(unsigned, __builtin_convertvector(v, bf16x2));
}
template <int CTRL> __device__ __forceinline__ float dpp(float x) {
  return __builtin_bit_cast(float, __builtin_amdgcn_mov_dpp(__builtin_bit_cast(int, x), CTRL, 0xf, 0xf, true));
}
__device__ __forceinline__ float xkg_max(float x) {
  auto s = __builtin_amdgcn_permlane16_swap(__float_as_uint(x), __float_as_uint(x), false, false);
  x = fmaxf(__uint_as_float(s[0]), __uint_as_float(s[1]));
  auto t = __builtin_amdgcn_permlane32_swap(__float_as_uint(x), __float_as_uint(x), false, false);
  return fmaxf(__uint_as_float(t[0]), __uint_as_float(t[1]));
}
__device__ __forceinline__ float xkg_sum(float x) {
  auto s = __builtin_amdgcn_permlane16_swap(__float_as_uint(x), __float_as_uint(x), false, false);
  x = __uint_as_float(s[0]) + __uint_as_float(s[1]);
  auto t = __builtin_amdgcn_permlane32_swap(__float_as_uint(x), __float_as_uint(x), false, false);
  return __uint_as_float(t[0]) + __uint_as_float(t[1]);
}
__device__ __forceinline__ int wave_incl_scan(int x) {
  x += __builtin_amdgcn_update_dpp(0, x, 0x111, 0xf, 0xf, false);
  x += __builtin_amdgcn_update_dpp(0, x, 0x112, 0xf, 0xf, false);
  x += __builtin_amdgcn_update_dpp(0, x, 0x114, 0xf, 0xf, false);
  x += __builtin_amdgcn_update_dpp(0, x, 0x118, 0xf, 0xf, false);
  x += __builtin_amdgcn_update_dpp(0, x, 0x142, 0xa, 0xf, false);
  x += __builtin_amdgcn_update_dpp(0, x, 0x143, 0xc, 0xf, false);
  return x;
}
__device__ __forceinline__ int wave_min(int x) {
  constexpr int BIG = 0x7fffffff;
  x = min(x, __builtin_amdgcn_update_dpp(BIG, x, 0x111, 0xf, 0xf, false));
  x = min(x, __builtin_amdgcn_update_dpp(BIG, x, 0x112, 0xf, 0xf, false));
  x = min(x, __builtin_amdgcn_update_dpp(BIG, x, 0x114, 0xf, 0xf, false));
  x = min(x, __builtin_amdgcn_update_dpp(BIG, x, 0x118, 0xf, 0xf, false));
  x = min(x, __builtin_amdgcn_update_dpp(BIG, x, 0x142, 0xa, 0xf, false));
  x = min(x, __builtin_amdgcn_update_dpp(BIG, x, 0x143, 0xc, 0xf, false));
  return __builtin_amdgcn_readlane(x, 63);
}
__device__ __forceinline__ int live_pages(int ctx) {
  if (ctx <= 0) return NB;
  ctx = min(ctx, NB * BS);
  return (ctx + BS - 1) / BS;
}
struct Geo { int npg, ustart, wstart, wtotal; bool sh; };
__device__ __forceinline__ Geo seg_geometry(int lane, int nb_l, int pn_incl, int Pc) {
  Geo g;
  g.sh = lane < NG;
  const int pn_tot = __builtin_amdgcn_readlane(pn_incl, 63);
  const int priv_before = (pn_incl - nb_l) - (lane - NG) * Pc;
  g.npg = g.sh ? Pc : (lane < NSEG ? nb_l - Pc : 0);
  g.ustart = g.sh ? lane * Pc : NG * Pc + priv_before;
  g.wstart = g.sh ? lane * Pc * WSHARED : NG * Pc * WSHARED + WPRIV * priv_before;
  g.wtotal = NG * Pc * WSHARED + WPRIV * (pn_tot - B * Pc);
  return g;
}
__device__ __forceinline__ int pos_to_page(int pos, int wstart, bool sh, int npg) {
  const int d = max(pos - wstart, 0);
  const int pgs = sh ? (d + WSHARED - 1) / WSHARED : (d + WPRIV - 1) / WPRIV;
  return min(pgs, npg);
}

struct KV { u32x4 k[8]; u32x4 v[8]; };

__device__ __forceinline__ void issue_k(KV& r, const __hip_bfloat16* kbase, int phys) {
  const __hip_bfloat16* p = kbase + (size_t)phys * PAGE_ELEMS;
#pragma unroll
  for (int c = 0; c < 8; ++c) r.k[c] = ldkv<(KV_NT & 1)>(p + c * 32);
}
__device__ __forceinline__ void issue_v(KV& r, const __hip_bfloat16* vbase, int phys) {
  const __hip_bfloat16* p = vbase + (size_t)phys * PAGE_ELEMS;
#pragma unroll
  for (int i = 0; i < 4; ++i)
#pragma unroll
    for (int h = 0; h < 2; ++h) r.v[i * 2 + h] = ldkv<(KV_NT >> 1)>(p + i * TOK_STRIDE + h * 128);
}

struct Ctx {
  const __hip_bfloat16* kbase; const __hip_bfloat16* vbase;
  unsigned char* lds; unsigned char* ws;
  int lane, wave, head, wgi, plist, nitems;
  unsigned mk[4];
};

template <int NR>
__device__ __forceinline__ void merge_rows(const Ctx& cx, float* po, float* ml, size_t slot0) {
  const float* mrg = reinterpret_cast<const float*>(cx.lds + LDS_MRG);
  const int lane = cx.lane;
  __syncthreads();
  if (cx.wave < NR) {
    const int row = cx.wave;
    float M = -INFINITY;
#pragma unroll
    for (int w = 0; w < NW; ++w) M = fmaxf(M, mrg[(w * MR * 2 + row) * MREC + 256]);
    float L = 0.f;
    f32x4 o = f32x4{0.f, 0.f, 0.f, 0.f};
#pragma unroll
    for (int w = 0; w < NW; ++w) {
      const float* rec = mrg + (w * MR * 2 + row) * MREC;
      const float lw = rec[257];
      const float wt = (lw > 0.f) ? __builtin_amdgcn_exp2f(rec[256] - M) : 0.f;
      L = fmaf(wt, lw, L);
      const f32x4 a = *reinterpret_cast<const f32x4*>(rec + 4 * lane);
      o += a * wt;
    }
    const size_t slot = slot0 + row;
    *reinterpret_cast<f32x4*>(po + slot * D + 4 * lane) = o;
    if (lane == 0) *reinterpret_cast<float2*>(ml + slot * 2) = make_float2(M, L);
  }
  __syncthreads();
}

__device__ __forceinline__ void run_private(const Ctx& cx, KV& r, int seq, int kb, int ke, int pg0, int ctxlen) {
  const int lane = cx.lane;
  float m = -INFINITY, l = 0.f;
  f32x4 a0 = f32x4{0.f, 0.f, 0.f, 0.f}, a1 = f32x4{0.f, 0.f, 0.f, 0.f};
  const unsigned char* qp = cx.lds + LDS_Q + seq * QS_STRIDE + ((lane >> 3) & 1) * QH_STRIDE + (lane >> 4) * 16;
  u32x4 qf[8];
#pragma unroll
  for (int c = 0; c < 8; ++c) qf[c] = *reinterpret_cast<const u32x4*>(qp + c * 64);
  const bool uni = ctxlen <= 0;
  const bool samehead = ((lane >> 5) & 1) == ((lane >> 3) & 1);
  for (int k = kb; k < ke; ++k) {
    const int pg = pg0 + 8 * (k - kb);
    const bool more = (k + 1) < cx.nitems;
    const int nphys = __builtin_amdgcn_readlane(cx.plist, more ? k + 1 : k);
    f32x4 sc = f32x4{0.f, 0.f, 0.f, 0.f};
#pragma unroll
    for (int c = 0; c < 8; ++c) sc = mfma(r.k[c], qf[c], sc);
    if (more) issue_k(r, cx.kbase, nphys);
    const int rem = uni ? BS : ctxlen - pg * BS;
    float t[4];
#pragma unroll
    for (int i = 0; i < 4; ++i) t[i] = uni ? 0.f : sc[i] * C2;
    if (rem < BS) {
#pragma unroll
      for (int i = 0; i < 4; ++i) t[i] = ((lane >> 4) * 4 + i < rem) ? t[i] : -INFINITY;
    }
    float pm = fmaxf(fmaxf(t[0], t[1]), fmaxf(t[2], t[3]));
    pm = xkg_max(pm);
    const float mn = fmaxf(m, pm);
    const float msub = (mn == -INFINITY) ? 0.f : mn;
    const float alpha = __builtin_amdgcn_exp2f(m - msub);
    m = mn;
    unsigned pp[4];
    float ps = 0.f;
#pragma unroll
    for (int i = 0; i < 4; ++i) {
      const float p = __builtin_amdgcn_exp2f(t[i] - msub);
      pp[i] = cvtpk(p, p);
      ps += __uint_as_float(pp[i] & 0xffff0000u);
    }
    l = fmaf(l, alpha, ps);
    const float aoth = dpp<0x128>(alpha);
    const float aa = samehead ? alpha : aoth;
    a0 *= aa; a1 *= aa;
#pragma unroll
    for (int i = 0; i < 4; ++i) {
      const u32x4 pa = u32x4{pp[i] & cx.mk[0], pp[i] & cx.mk[1], pp[i] & cx.mk[2], pp[i] & cx.mk[3]};
      a0 = mfma(pa, r.v[i * 2 + 0], a0);
      a1 = mfma(pa, r.v[i * 2 + 1], a1);
    }
    if (more) issue_v(r, cx.vbase, nphys);
  }
  float* mrg = reinterpret_cast<float*>(cx.lds + LDS_MRG);
  const float lt = xkg_sum(l);
  float* rec = mrg + (cx.wave * MR * 2 + (lane >> 5)) * MREC + 8 * (lane & 15) + 4 * ((lane >> 4) & 1);
  *reinterpret_cast<f32x4*>(rec) = a0;
  *reinterpret_cast<f32x4*>(rec + 128) = a1;
  if ((lane & 55) == 0) {
    float* rr = mrg + (cx.wave * MR * 2 + (lane >> 3)) * MREC + 256;
    rr[0] = m; rr[1] = lt;
  }
  merge_rows<2>(cx, reinterpret_cast<float*>(cx.ws + WS_PO_PR), reinterpret_cast<float*>(cx.ws + WS_ML_PR),
                ((size_t)(cx.head * B + seq) * NWG + cx.wgi) * 2);
}

template <int N, int RM, int BM> __device__ __forceinline__ unsigned bcast(unsigned old, unsigned x) {
  return (unsigned)__builtin_amdgcn_update_dpp((int)old, (int)x, 0x150 + N, RM, BM, false);
}
template <int SQ> __device__ __forceinline__ void shared_pv(const Ctx& cx, const KV& r, unsigned p01, unsigned p23, float alpha,
                                                            const unsigned (&sel)[8], f32x4& a0, f32x4& a1) {
  unsigned x01 = bcast<2 * SQ, 0xf, 0x3>(0u, p01); x01 = bcast<2 * SQ + 1, 0xf, 0xc>(x01, p01);
  unsigned x23 = bcast<2 * SQ, 0xf, 0x3>(0u, p23); x23 = bcast<2 * SQ + 1, 0xf, 0xc>(x23, p23);
  unsigned au = bcast<2 * SQ, 0x3, 0xf>(0u, __float_as_uint(alpha)); au = bcast<2 * SQ + 1, 0xc, 0xf>(au, __float_as_uint(alpha));
  const float aa = __uint_as_float(au);
  a0 *= aa; a1 *= aa;
#pragma unroll
  for (int i = 0; i < 4; ++i) {
    const unsigned x = i < 2 ? x01 : x23;
    const u32x4 pa = u32x4{__builtin_amdgcn_perm(x, x, sel[(i & 1) * 4 + 0]), __builtin_amdgcn_perm(x, x, sel[(i & 1) * 4 + 1]),
                           __builtin_amdgcn_perm(x, x, sel[(i & 1) * 4 + 2]), __builtin_amdgcn_perm(x, x, sel[(i & 1) * 4 + 3])};
    a0 = mfma(pa, r.v[i * 2 + 0], a0);
    a1 = mfma(pa, r.v[i * 2 + 1], a1);
  }
}
__device__ __forceinline__ void run_shared(const Ctx& cx, KV& r, int grp, int kb, int ke) {
  static_assert(S == 8, "16 MFMA columns = 8 sequences x 2 heads");
  const int lane = cx.lane;
  float m = -INFINITY, l = 0.f;
  f32x4 acc[S][2];
#pragma unroll
  for (int s = 0; s < S; ++s) { acc[s][0] = f32x4{0.f, 0.f, 0.f, 0.f}; acc[s][1] = f32x4{0.f, 0.f, 0.f, 0.f}; }
  const unsigned char* qp = cx.lds + LDS_Q + (grp * S + ((lane & 15) >> 1)) * QS_STRIDE + (lane & 1) * QH_STRIDE + (lane >> 4) * 16;
  u32x4 qf[8];
#pragma unroll
  for (int c = 0; c < 8; ++c) qf[c] = *reinterpret_cast<const u32x4*>(qp + c * 64);
  unsigned sel[8];
#pragma unroll
  for (int hf = 0; hf < 2; ++hf)
#pragma unroll
    for (int d = 0; d < 4; ++d) {
      const unsigned src = hf ? 0x0302u : 0x0100u;
      sel[hf * 4 + d] = (((lane & 7) >> 1) == d) ? ((lane & 1) ? (0x00000c0cu | (src << 16)) : (0x0c0c0000u | src)) : 0x0c0c0c0cu;
    }
  for (int k = kb; k < ke; ++k) {
    const bool more = (k + 1) < cx.nitems;
    const int nphys = __builtin_amdgcn_readlane(cx.plist, more ? k + 1 : k);
    f32x4 sc = f32x4{0.f, 0.f, 0.f, 0.f};
#pragma unroll
    for (int c = 0; c < 8; ++c) sc = mfma(r.k[c], qf[c], sc);
    if (more) issue_k(r, cx.kbase, nphys);
    float t[4];
#pragma unroll
    for (int i = 0; i < 4; ++i) t[i] = sc[i] * C2;
    float pm = fmaxf(fmaxf(t[0], t[1]), fmaxf(t[2], t[3]));
    pm = xkg_max(pm);
    const float mn = fmaxf(m, pm);
    const float msub = (mn == -INFINITY) ? 0.f : mn;
    const float alpha = __builtin_amdgcn_exp2f(m - msub);
    m = mn;
    const unsigned p01 = cvtpk(__builtin_amdgcn_exp2f(t[0] - msub), __builtin_amdgcn_exp2f(t[1] - msub));
    const unsigned p23 = cvtpk(__builtin_amdgcn_exp2f(t[2] - msub), __builtin_amdgcn_exp2f(t[3] - msub));
    const float ps = (__uint_as_float(p01 << 16) + __uint_as_float(p01 & 0xffff0000u)) +
                     (__uint_as_float(p23 << 16) + __uint_as_float(p23 & 0xffff0000u));
    l = fmaf(l, alpha, ps);
    shared_pv<0>(cx, r, p01, p23, alpha, sel, acc[0][0], acc[0][1]);
    shared_pv<1>(cx, r, p01, p23, alpha, sel, acc[1][0], acc[1][1]);
    shared_pv<2>(cx, r, p01, p23, alpha, sel, acc[2][0], acc[2][1]);
    shared_pv<3>(cx, r, p01, p23, alpha, sel, acc[3][0], acc[3][1]);
    shared_pv<4>(cx, r, p01, p23, alpha, sel, acc[4][0], acc[4][1]);
    shared_pv<5>(cx, r, p01, p23, alpha, sel, acc[5][0], acc[5][1]);
    shared_pv<6>(cx, r, p01, p23, alpha, sel, acc[6][0], acc[6][1]);
    shared_pv<7>(cx, r, p01, p23, alpha, sel, acc[7][0], acc[7][1]);
    if (more) issue_v(r, cx.vbase, nphys);
  }
  float* mrg = reinterpret_cast<float*>(cx.lds + LDS_MRG);
  const float lt = xkg_sum(l);
#pragma unroll
  for (int rd = 0; rd < S / MR; ++rd) {
#pragma unroll
    for (int ss = 0; ss < MR; ++ss) {
      float* rec = mrg + ((cx.wave * MR + ss) * 2 + (lane >> 5)) * MREC + 8 * (lane & 15) + 4 * ((lane >> 4) & 1);
      *reinterpret_cast<f32x4*>(rec) = acc[rd * MR + ss][0];
      *reinterpret_cast<f32x4*>(rec + 128) = acc[rd * MR + ss][1];
    }
    if ((lane >> 3) == rd) {
      float* rr = mrg + (cx.wave * MR * 2 + (lane & 7)) * MREC + 256;
      rr[0] = m; rr[1] = lt;
    }
    merge_rows<2 * MR>(cx, reinterpret_cast<float*>(cx.ws + WS_PO_SH), reinterpret_cast<float*>(cx.ws + WS_ML_SH),
                       ((size_t)(cx.head * NG + grp) * NWG + cx.wgi) * (2 * S) + rd * 2 * MR);
  }
}

__global__ void __launch_bounds__(NTHR, 2)
attn_main(const __hip_bfloat16* __restrict__ q, const __hip_bfloat16* __restrict__ kc, const __hip_bfloat16* __restrict__ vc,
          const int* __restrict__ bt, const int* __restrict__ ctx_lens, unsigned char* __restrict__ ws) {
  __shared__ __attribute__((aligned(16))) unsigned char lds[LDS_BYTES];
  const int tid = threadIdx.x, lane = tid & 63, wave = __builtin_amdgcn_readfirstlane(tid >> 6);
  const int head = blockIdx.x & 7, wgi = blockIdx.x >> 3;
  int* smin = reinterpret_cast<int*>(lds + LDS_MISC);
  int2 tb[4];
#pragma unroll
  for (int i = 0; i < 4; ++i) tb[i] = *reinterpret_cast<const int2*>(bt + (wave * 4 + i) * NB + 2 * lane);
  const int2 z = *reinterpret_cast<const int2*>(bt + 2 * lane);
  const bool seqlane = lane >= NG && lane < NSEG;
  const int myctx = ctx_lens[seqlane ? lane - NG : 0];
  const int qrow = tid >> 3, qpc = tid & 7;
  const __hip_bfloat16* qsrc = q + ((size_t)(qrow >> 1) * H + head * 2 + (qrow & 1)) * D + qpc * 32;
  u32x4 qv[4];
#pragma unroll
  for (int i = 0; i < 4; ++i) qv[i] = *reinterpret_cast<const u32x4*>(qsrc + i * 8);
  const int nb_l = seqlane ? live_pages(myctx) : 0;
  const int pn_incl = wave_incl_scan(nb_l);
  const int fullmin = wave_min(seqlane ? (myctx <= 0 ? 0 : min(myctx, NB * BS) / BS) : NB);
  {
    bool ne = false, no = false;
#pragma unroll
    for (int i = 0; i < 4; ++i) {
      ne |= tb[i].x != z.x; no |= tb[i].y != z.y;
      *reinterpret_cast<int2*>(lds + LDS_BT + ((wave * 4 + i) * NB + 2 * lane) * 4) = tb[i];
    }
    const unsigned long long me = __ballot(ne), mo = __ballot(no);
    const int fe = me ? 2 * __builtin_ctzll(me) : NB, fo = mo ? 2 * __builtin_ctzll(mo) + 1 : NB;
    if (lane == 0) smin[wave] = min(fe, fo);
  }
  __syncthreads();
  int Pc = fullmin;
#pragma unroll
  for (int w = 0; w < NW; ++w) Pc = min(Pc, smin[w]);
  Pc = __builtin_amdgcn_readfirstlane(Pc);
  if (blockIdx.x == 0 && tid == 0) *reinterpret_cast<int*>(ws + WS_PLAN) = Pc;
  const Geo ge = seg_geometry(lane, nb_l, pn_incl, Pc);
  const int lo = (int)(((long long)ge.wtotal * wgi) / NWG), hi = (int)(((long long)ge.wtotal * (wgi + 1)) / NWG);
  const int pb = pos_to_page(lo, ge.wstart, ge.sh, ge.npg), pe = pos_to_page(hi, ge.wstart, ge.sh, ge.npg);
  const int cnt = pe - pb;
  unsigned long long segmask = __ballot(cnt > 0);
  int off = 0, total = 0;
  if (segmask) {
    const int jf = __builtin_ctzll(segmask), jl = 63 - __builtin_clzll(segmask);
    const int u_lo = __builtin_amdgcn_readlane(ge.ustart + pb, jf);
    off = ge.ustart + pb - u_lo;
    total = __builtin_amdgcn_readlane(ge.ustart + pe, jl) - u_lo;
  }
  const int g = wave + 8 * lane;
  int plist = 0;
  {
    unsigned long long mm = segmask;
    while (mm) {
      const int j = __builtin_ctzll(mm); mm &= mm - 1;
      const int o_ = __builtin_amdgcn_readlane(off, j), c_ = __builtin_amdgcn_readlane(cnt, j), b_ = __builtin_amdgcn_readlane(pb, j);
      const int row = j < NG ? j * S : j - NG;
      const int base = j < NG ? 0 : Pc;
      if (g >= o_ && g < o_ + c_) plist = reinterpret_cast<const int*>(lds + LDS_BT)[row * NB + base + b_ + (g - o_)];
    }
  }
  Ctx cx;
  cx.lane = lane; cx.wave = wave; cx.head = head; cx.wgi = wgi; cx.lds = lds; cx.ws = ws; cx.plist = plist;
  cx.nitems = __builtin_amdgcn_readfirstlane(total > wave ? (total - wave + 7) >> 3 : 0);
  cx.kbase = kc + head * D + (lane & 15) * TOK_STRIDE + (lane >> 4) * 8;
  cx.vbase = vc + head * D + (lane >> 4) * 4 * TOK_STRIDE + (lane & 15) * 8;
#pragma unroll
  for (int d = 0; d < 4; ++d) cx.mk[d] = (((lane & 7) >> 1) == d) ? ((lane & 1) ? 0xffff0000u : 0x0000ffffu) : 0u;
  KV r;
  if (cx.nitems > 0) {
    const int ph = __builtin_amdgcn_readlane(plist, 0);
    issue_k(r, cx.kbase, ph);
    issue_v(r, cx.vbase, ph);
  } else {
#pragma unroll
    for (int i = 0; i < 8; ++i) { r.k[i] = u32x4{0u, 0u, 0u, 0u}; r.v[i] = u32x4{0u, 0u, 0u, 0u}; }
  }
  {
    unsigned char* qd = lds + LDS_Q + (qrow >> 1) * QS_STRIDE + (qrow & 1) * QH_STRIDE + qpc * 64;
#pragma unroll
    for (int i = 0; i < 4; ++i) *reinterpret_cast<u32x4*>(qd + i * 16) = qv[i];
  }
  __syncthreads();
  while (segmask) {
    const int j = __builtin_ctzll(segmask); segmask &= segmask - 1;
    const int o_ = __builtin_amdgcn_readlane(off, j), c_ = __builtin_amdgcn_readlane(cnt, j), b_ = __builtin_amdgcn_readlane(pb, j);
    const int kb = (o_ - wave + 7) >> 3, ke = (o_ + c_ - wave + 7) >> 3;
    const int pgrel = b_ + (wave + 8 * kb - o_);
    if (j < NG) {
      run_shared(cx, r, j, kb, ke);
    } else {
      const int cl = __builtin_amdgcn_readlane(myctx, j);
      run_private(cx, r, j - NG, kb, ke, Pc + pgrel, min(cl, NB * BS));
    }
  }
}

__global__ void __launch_bounds__(256)
attn_combine(const int* __restrict__ ctx_lens, const unsigned char* __restrict__ ws, __hip_bfloat16* __restrict__ out) {
  const int lane = threadIdx.x & 63, wave = threadIdx.x >> 6;
  const int rowid = blockIdx.x * 4 + wave;
  const int b = rowid >> 4, h = rowid & 15, head = h >> 1, hh = h & 1;
  const bool seqlane = lane >= NG && lane < NSEG;
  const int myctx = ctx_lens[seqlane ? lane - NG : 0];
  const int Pc = *reinterpret_cast<const int*>(ws + WS_PLAN);
  const int nb_l = seqlane ? live_pages(myctx) : 0;
  const Geo ge = seg_geometry(lane, nb_l, wave_incl_scan(nb_l), Pc);
  const int wgi = lane & 31;
  const bool sh = lane < 32;
  const int bu = __builtin_amdgcn_readfirstlane(b);
  const int s_npg = sh ? Pc : __builtin_amdgcn_readlane(ge.npg, NG + bu);
  const int s_ws = sh ? __builtin_amdgcn_readlane(ge.wstart, bu / S) : __builtin_amdgcn_readlane(ge.wstart, NG + bu);
  const int lo = (int)(((long long)ge.wtotal * wgi) / NWG), hi = (int)(((long long)ge.wtotal * (wgi + 1)) / NWG);
  const bool has = pos_to_page(hi, s_ws, sh, s_npg) > pos_to_page(lo, s_ws, sh, s_npg);
  size_t slot;
  const float* mlp;
  if (lane < 32) {
    slot = ((size_t)(head * NG + b / S) * NWG + wgi) * (2 * S) + ((b % S) * 2 + hh);
    mlp = reinterpret_cast<const float*>(ws + WS_ML_SH) + slot * 2;
  } else {
    slot = ((size_t)(head * B + b) * NWG + wgi) * 2 + hh;
    mlp = reinterpret_cast<const float*>(ws + WS_ML_PR) + slot * 2;
  }
  float2 ml = make_float2(-INFINITY, 0.f);
  if (has) ml = *reinterpret_cast<const float2*>(mlp);
  unsigned long long mask = __ballot(has);
  float M = ml.x;
#pragma unroll
  for (int o = 32; o > 0; o >>= 1) M = fmaxf(M, __shfl_xor(M, o, 64));
  const float w = (has && ml.y > 0.f) ? __builtin_amdgcn_exp2f(ml.x - M) : 0.f;
  float L = w * ml.y;
#pragma unroll
  for (int o = 32; o > 0; o >>= 1) L += __shfl_xor(L, o, 64);
  const unsigned slo = (unsigned)slot;
  f32x4 o = f32x4{0.f, 0.f, 0.f, 0.f};
  while (mask) {
    const int j = __builtin_ctzll(mask); mask &= mask - 1;
    const unsigned sl = __builtin_amdgcn_readlane(slo, j);
    const float wj = __builtin_bit_cast(float, __builtin_amdgcn_readlane(__builtin_bit_cast(int, w), j));
    const float* po = reinterpret_cast<const float*>(ws + (j < 32 ? WS_PO_SH : WS_PO_PR)) + (size_t)sl * D + 4 * lane;
    const f32x4 a = *reinterpret_cast<const f32x4*>(po);
    o += a * wj;
  }
  const float inv = (L > 0.f) ? 1.f / L : 0.f;
  uint2 pk;
  pk.x = cvtpk(o[0] * inv, o[1] * inv);
  pk.y = cvtpk(o[2] * inv, o[3] * inv);
  *reinterpret_cast<uint2*>(out + (size_t)rowid * D + 4 * lane) = pk;
}
}

extern "C" void kernel_launch(void* const* d_in, const int* in_sizes, int n_in, void* d_out, int out_size, void* d_ws,
                              size_t ws_size, hipStream_t stream) {
  const __hip_bfloat16* q = (const __hip_bfloat16*)d_in[0];
  const __hip_bfloat16* kc = (const __hip_bfloat16*)d_in[1];
  const __hip_bfloat16* vc = (const __hip_bfloat16*)d_in[2];
  const int* bt = (const int*)d_in[3];
  const int* ctx = (const int*)d_in[4];
  attn_main<<<HKV * NWG, NTHR, 0, stream>>>(q, kc, vc, bt, ctx, (unsigned char*)d_ws);
  attn_combine<<<B * H / 4, 256, 0, stream>>>(ctx, (const unsigned char*)d_ws, (__hip_bfloat16*)d_out);
}
```

```cpp
#include <hip/hip_runtime.h>
#include <hip/hip_bf16.h>
#include <cstdint>

#define SGRP 8
#define WSH 5
#define KAUX 0
#define KVL 1
#define KPAUX 2
#define VSAUX 0
#define VAUX 2

namespace {
constexpr int B = 32, H = 16, HKV = 8, D = 256, BS = 16, NB = 128;
constexpr int S = SGRP, NG = B / S;
constexpr int NWG = 32;
constexpr int NW = 8, NTHR = NW * 64;
constexpr int WPRIV = 4, WSHARED = WSH;
constexpr int NSEG = NG + B;
constexpr float C2 = 0.0625f * 1.4426950408889634f;
constexpr int PAGE_ELEMS = BS * HKV * D;
constexpr int TOK_STRIDE = HKV * D;

constexpr size_t WS_PLAN = 0;
constexpr size_t WS_ML = 4096;
constexpr size_t WS_PO_SH = WS_ML + (size_t)B * H * NWG * 2 * 8;
constexpr size_t WS_PO_PR = WS_PO_SH + (size_t)HKV * NG * NWG * 2 * S * D * 4;
constexpr size_t WS_END = WS_PO_PR + (size_t)HKV * B * NWG * 2 * D * 4;

constexpr int QH_STRIDE = 576;
constexpr int QS_STRIDE = 2 * QH_STRIDE;
constexpr int LDS_BT = 0;
constexpr int LDS_Q = LDS_BT + B * NB * 4;
constexpr int MR = 4;
constexpr int MREC = 264;
constexpr int LDS_MRG = LDS_Q + B * QS_STRIDE;
constexpr int LDS_MISC = LDS_MRG + NW * MR * 2 * MREC * 4;
constexpr int LDS_BYTES = LDS_MISC + 64;

using bf16x8 = __attribute__((ext_vector_type(8))) __bf16;
using u32x4 = __attribute__((ext_vector_type(4))) unsigned;
using i32x4 = __attribute__((ext_vector_type(4))) int;
using f32x4 = __attribute__((ext_vector_type(4))) float;

using rsrc_t = __amdgpu_buffer_rsrc_t;
template <int AUX> __device__ __forceinline__ u32x4 ldkv(rsrc_t r, unsigned voff, unsigned soff) {
  return __builtin_amdgcn_raw_buffer_load_b128(r, voff, soff, AUX);
}
__device__ __forceinline__ f32x4 mfma(u32x4 a, u32x4 b, f32x4 c) {
  return __builtin_amdgcn_mfma_f32_16x16x32_bf16(__builtin_bit_cast(bf16x8, a), __builtin_bit_cast(bf16x8, b), c, 0, 0, 0);
}
__device__ __forceinline__ unsigned cvtpk(float lo, float hi) {
  using f32x2 = __attribute__((ext_vector_type(2))) float;
  using bf16x2 = __attribute__((ext_vector_type(2))) __bf16;
  const f32x2 v = {lo, hi};
  return __builtin_bit_cast(unsigned, __builtin_convertvector(v, bf16x2));
}
template <int CTRL> __device__ __forceinline__ float dpp(float x) {
  return __builtin_bit_cast(float, __builtin_amdgcn_mov_dpp(__builtin_bit_cast(int, x), CTRL, 0xf, 0xf, true));
}
__device__ __forceinline__ float xkg_max(float x) {
  auto s = __builtin_amdgcn_permlane16_swap(__float_as_uint(x), __float_as_uint(x), false, false);
  x = fmaxf(__uint_as_float(s[0]), __uint_as_float(s[1]));
  auto t = __builtin_amdgcn_permlane32_swap(__float_as_uint(x), __float_as_uint(x), false, false);
  return fmaxf(__uint_as_float(t[0]), __uint_as_float(t[1]));
}
__device__ __forceinline__ float xkg_sum(float x) {
  auto s = __builtin_amdgcn_permlane16_swap(__float_as_uint(x), __float_as_uint(x), false, false);
  x = __uint_as_float(s[0]) + __uint_as_float(s[1]);
  auto t = __builtin_amdgcn_permlane32_swap(__float_as_uint(x), __float_as_uint(x), false, false);
  return __uint_as_float(t[0]) + __uint_as_float(t[1]);
}
__device__ __forceinline__ int wave_incl_scan(int x) {
  x += __builtin_amdgcn_update_dpp(0, x, 0x111, 0xf, 0xf, false);
  x += __builtin_amdgcn_update_dpp(0, x, 0x112, 0xf, 0xf, false);
  x += __builtin_amdgcn_update_dpp(0, x, 0x114, 0xf, 0xf, false);
  x += __builtin_amdgcn_update_dpp(0, x, 0x118, 0xf, 0xf, false);
  x += __builtin_amdgcn_update_dpp(0, x, 0x142, 0xa, 0xf, false);
  x += __builtin_amdgcn_update_dpp(0, x, 0x143, 0xc, 0xf, false);
  return x;
}
__device__ __forceinline__ int wave_min(int x) {
  constexpr int BIG = 0x7fffffff;
  x = min(x, __builtin_amdgcn_update_dpp(BIG, x, 0x111, 0xf, 0xf, false));
  x = min(x, __builtin_amdgcn_update_dpp(BIG, x, 0x112, 0xf, 0xf, false));
  x = min(x, __builtin_amdgcn_update_dpp(BIG, x, 0x114, 0xf, 0xf, false));
  x = min(x, __builtin_amdgcn_update_dpp(BIG, x, 0x118, 0xf, 0xf, false));
  x = min(x, __builtin_amdgcn_update_dpp(BIG, x, 0x142, 0xa, 0xf, false));
  x = min(x, __builtin_amdgcn_update_dpp(BIG, x, 0x143, 0xc, 0xf, false));
  return __builtin_amdgcn_readlane(x, 63);
}
template <int CTRL, int RM> __device__ __forceinline__ float dppo(float old, float x) {
  return __builtin_bit_cast(float, __builtin_amdgcn_update_dpp(__builtin_bit_cast(int, old), __builtin_bit_cast(int, x), CTRL, RM, 0xf, false));
}
__device__ __forceinline__ float wave_fmax(float x) {
  const float NI = -INFINITY;
  x = fmaxf(x, dppo<0x111, 0xf>(NI, x)); x = fmaxf(x, dppo<0x112, 0xf>(NI, x));
  x = fmaxf(x, dppo<0x114, 0xf>(NI, x)); x = fmaxf(x, dppo<0x118, 0xf>(NI, x));
  x = fmaxf(x, dppo<0x142, 0xa>(NI, x)); x = fmaxf(x, dppo<0x143, 0xc>(NI, x));
  return __builtin_bit_cast(float, __builtin_amdgcn_readlane(__builtin_bit_cast(int, x), 63));
}
__device__ __forceinline__ float wave_fsum(float x) {
  x += dppo<0x111, 0xf>(0.f, x); x += dppo<0x112, 0xf>(0.f, x);
  x += dppo<0x114, 0xf>(0.f, x); x += dppo<0x118, 0xf>(0.f, x);
  x += dppo<0x142, 0xa>(0.f, x); x += dppo<0x143, 0xc>(0.f, x);
  return __builtin_bit_cast(float, __builtin_amdgcn_readlane(__builtin_bit_cast(int, x), 63));
}
__device__ __forceinline__ int live_pages(int ctx) {
  if (ctx <= 0) return NB;
  ctx = min(ctx, NB * BS);
  return (ctx + BS - 1) / BS;
}
struct Geo { int npg, ustart, wstart, wtotal; bool sh; };
__device__ __forceinline__ Geo seg_geometry(int lane, int nb_l, int pn_incl, int Pc) {
  Geo g;
  g.sh = lane < NG;
  const int pn_tot = __builtin_amdgcn_readlane(pn_incl, 63);
  const int priv_before = (pn_incl - nb_l) - (lane - NG) * Pc;
  g.npg = g.sh ? Pc : (lane < NSEG ? nb_l - Pc : 0);
  g.ustart = g.sh ? lane * Pc : NG * Pc + priv_before;
  g.wstart = g.sh ? lane * Pc * WSHARED : NG * Pc * WSHARED + WPRIV * priv_before;
  g.wtotal = NG * Pc * WSHARED + WPRIV * (pn_tot - B * Pc);
  return g;
}
__device__ __forceinline__ int pos_to_page(int pos, int wstart, bool sh, int npg) {
  const int d = max(pos - wstart, 0);
  const int pgs = sh ? (d + WSHARED - 1) / WSHARED : (d + WPRIV - 1) / WPRIV;
  return min(pgs, npg);
}

struct KV { u32x4 k[8]; u32x4 v[8]; };

struct Src { rsrc_t rs; unsigned off; };
__device__ __forceinline__ void issue_k(KV& r, const Src& k, int phys) {
  const unsigned so = (unsigned)phys * (PAGE_ELEMS * 2);
#pragma unroll
  for (int c = 0; c < 8; ++c) r.k[c] = ldkv<KAUX>(k.rs, k.off + c * 64, so);
}
template <int AUX> __device__ __forceinline__ void issue_v(KV& r, const Src& v, int phys) {
  const unsigned so = (unsigned)phys * (PAGE_ELEMS * 2);
#pragma unroll
  for (int i = 0; i < 4; ++i)
#pragma unroll
    for (int h = 0; h < 2; ++h) r.v[i * 2 + h] = ldkv<AUX>(v.rs, v.off + h * 256, so + i * (TOK_STRIDE * 2));
}

__device__ __forceinline__ void issue_kl(KV& r, const Src& k, const Src& v, int phys) {
  const unsigned so = (unsigned)phys * (PAGE_ELEMS * 2);
#pragma unroll
  for (int i = 0; i < 4; ++i)
#pragma unroll
    for (int h = 0; h < 2; ++h) r.k[i * 2 + h] = ldkv<KPAUX>(k.rs, v.off + h * 256, so + i * (TOK_STRIDE * 2));
}
__device__ __forceinline__ float dot8(u32x4 a, u32x4 b, float c) {
  using bf16x2 = __attribute__((ext_vector_type(2))) __bf16;
#pragma unroll
  for (int d = 0; d < 4; ++d) {
    const unsigned x = a[d], y = b[d];
    c = __builtin_amdgcn_fdot2_f32_bf16(__builtin_bit_cast(bf16x2, x), __builtin_bit_cast(bf16x2, y), c, false);
  }
  return c;
}

struct Ctx {
  Src kbase, vbase;
  int nsh;
  unsigned char* lds; unsigned char* ws;
  int lane, wave, head, wgi, plist, nitems;
  unsigned mk[4];
};

template <int NR>
__device__ __forceinline__ void merge_rows(const Ctx& cx, float* po, size_t slot0, int seq0, int mlsub) {
  const float* mrg = reinterpret_cast<const float*>(cx.lds + LDS_MRG);
  const int lane = cx.lane;
  __syncthreads();
  if (cx.wave < NR) {
    const int row = cx.wave;
    float M = -INFINITY;
#pragma unroll
    for (int w = 0; w < NW; ++w) M = fmaxf(M, mrg[(w * MR * 2 + row) * MREC + 256]);
    float L = 0.f;
    f32x4 o = f32x4{0.f, 0.f, 0.f, 0.f};
#pragma unroll
    for (int w = 0; w < NW; ++w) {
      const float* rec = mrg + (w * MR * 2 + row) * MREC;
      const float lw = rec[257];
      const float wt = (lw > 0.f) ? __builtin_amdgcn_exp2f(rec[256] - M) : 0.f;
      L = fmaf(wt, lw, L);
      const f32x4 a = *reinterpret_cast<const f32x4*>(rec + 4 * lane);
      o += a * wt;
    }
    const size_t slot = slot0 + row;
    *reinterpret_cast<f32x4*>(po + slot * D + 4 * lane) = o;
    if (lane == 0)
      reinterpret_cast<float2*>(cx.ws + WS_ML)[((seq0 + (row >> 1)) * H + cx.head * 2 + (row & 1)) * (2 * NWG) + mlsub] = make_float2(M, L);
  }
  __syncthreads();
}

__device__ __forceinline__ void run_private(const Ctx& cx, KV& r, int seq, int kb, int ke, int pg0, int ctxlen, int piece) {
  const int lane = cx.lane;
  float m = -INFINITY, l = 0.f;
  f32x4 a0 = f32x4{0.f, 0.f, 0.f, 0.f}, a1 = f32x4{0.f, 0.f, 0.f, 0.f};
  const unsigned char* qp = cx.lds + LDS_Q + seq * QS_STRIDE + (lane & 15) * 16;
  u32x4 qm[2], qo[2];
#pragma unroll
  for (int h = 0; h < 2; ++h) {
    qm[h] = *reinterpret_cast<const u32x4*>(qp + ((lane >> 3) & 1) * QH_STRIDE + h * 256);
    qo[h] = *reinterpret_cast<const u32x4*>(qp + (((lane >> 3) & 1) ^ 1) * QH_STRIDE + h * 256);
  }
  const bool uni = ctxlen <= 0;
  const bool samehead = ((lane >> 5) & 1) == ((lane >> 3) & 1);
  for (int k = kb; k < ke; ++k) {
    const int pg = pg0 + 8 * (k - kb);
    const bool more = (k + 1) < cx.nitems;
    const int nphys = __builtin_amdgcn_readlane(cx.plist, more ? k + 1 : k);
    f32x4 sc = f32x4{0.f, 0.f, 0.f, 0.f};
    f32x4 sg;
#pragma unroll
    for (int i = 0; i < 4; ++i) {
      sc[i] = dot8(r.k[i * 2 + 1], qm[1], dot8(r.k[i * 2], qm[0], 0.f));
      sg[i] = dot8(r.k[i * 2 + 1], qo[1], dot8(r.k[i * 2], qo[0], 0.f));
    }
    if (more) issue_kl(r, cx.kbase, cx.vbase, nphys);
#pragma unroll
    for (int i = 0; i < 4; ++i) {
      float x = sc[i] + dpp<0x128>(sg[i]);
      x += dpp<0xB1>(x);
      x += dpp<0x4E>(x);
      x += dpp<0x141>(x);
      sc[i] = x;
    }
    const int rem = uni ? BS : ctxlen - pg * BS;
    float t[4];
#pragma unroll
    for (int i = 0; i < 4; ++i) t[i] = uni ? 0.f : sc[i] * C2;
    if (rem < BS) {
#pragma unroll
      for (int i = 0; i < 4; ++i) t[i] = ((lane >> 4) * 4 + i < rem) ? t[i] : -INFINITY;
    }
    float pm = fmaxf(fmaxf(t[0], t[1]), fmaxf(t[2], t[3]));
    pm = xkg_max(pm);
    const float mn = fmaxf(m, pm);
    const float msub = (mn == -INFINITY) ? 0.f : mn;
    const float alpha = __builtin_amdgcn_exp2f(m - msub);
    m = mn;
    unsigned pp[4];
    float ps = 0.f;
#pragma unroll
    for (int i = 0; i < 4; ++i) {
      const float p = __builtin_amdgcn_exp2f(t[i] - msub);
      pp[i] = cvtpk(p, p);
      ps += __uint_as_float(pp[i] & 0xffff0000u);
    }
    l = fmaf(l, alpha, ps);
    const float aoth = dpp<0x128>(alpha);
    const float aa = samehead ? alpha : aoth;
    a0 *= aa; a1 *= aa;
#pragma unroll
    for (int i = 0; i < 4; ++i) {
      const u32x4 pa = u32x4{pp[i] & cx.mk[0], pp[i] & cx.mk[1], pp[i] & cx.mk[2], pp[i] & cx.mk[3]};
      a0 = mfma(pa, r.v[i * 2 + 0], a0);
      a1 = mfma(pa, r.v[i * 2 + 1], a1);
    }
    if (more) issue_v<VAUX>(r, cx.vbase, nphys);
  }
  float* mrg = reinterpret_cast<float*>(cx.lds + LDS_MRG);
  const float lt = xkg_sum(l);
  float* rec = mrg + (cx.wave * MR * 2 + (lane >> 5)) * MREC + 8 * (lane & 15) + 4 * ((lane >> 4) & 1);
  *reinterpret_cast<f32x4*>(rec) = a0;
  *reinterpret_cast<f32x4*>(rec + 128) = a1;
  if ((lane & 55) == 0) {
    float* rr = mrg + (cx.wave * MR * 2 + (lane >> 3)) * MREC + 256;
    rr[0] = m; rr[1] = lt;
  }
  merge_rows<2>(cx, reinterpret_cast<float*>(cx.ws + WS_PO_PR), ((size_t)(cx.head * B + seq) * NWG + piece) * 2, seq, 2 * piece + 1);
}

template <int N, int RM, int BM> __device__ __forceinline__ unsigned bcast(unsigned old, unsigned x) {
  return (unsigned)__builtin_amdgcn_update_dpp((int)old, (int)x, 0x150 + N, RM, BM, false);
}
template <int SQ> __device__ __forceinline__ void shared_pv(const Ctx& cx, const KV& r, unsigned p01, unsigned p23, float alpha,
                                                            const unsigned (&sel)[8], f32x4& a0, f32x4& a1) {
  unsigned x01 = bcast<2 * SQ, 0xf, 0x3>(0u, p01); x01 = bcast<2 * SQ + 1, 0xf, 0xc>(x01, p01);
  unsigned x23 = bcast<2 * SQ, 0xf, 0x3>(0u, p23); x23 = bcast<2 * SQ + 1, 0xf, 0xc>(x23, p23);
  unsigned au = bcast<2 * SQ, 0x3, 0xf>(0u, __float_as_uint(alpha)); au = bcast<2 * SQ + 1, 0xc, 0xf>(au, __float_as_uint(alpha));
  const float aa = __uint_as_float(au);
  a0 *= aa; a1 *= aa;
#pragma unroll
  for (int i = 0; i < 4; ++i) {
    const unsigned x = i < 2 ? x01 : x23;
    const u32x4 pa = u32x4{__builtin_amdgcn_perm(x, x, sel[(i & 1) * 4 + 0]), __builtin_amdgcn_perm(x, x, sel[(i & 1) * 4 + 1]),
                           __builtin_amdgcn_perm(x, x, sel[(i & 1) * 4 + 2]), __builtin_amdgcn_perm(x, x, sel[(i & 1) * 4 + 3])};
    a0 = mfma(pa, r.v[i * 2 + 0], a0);
    a1 = mfma(pa, r.v[i * 2 + 1], a1);
  }
}
__device__ __forceinline__ void run_shared(const Ctx& cx, KV& r, int grp, int kb, int ke, int piece) {
  static_assert(S == 8, "16 MFMA columns = 8 sequences x 2 heads");
  const int lane = cx.lane;
  float m = -INFINITY, l = 0.f;
  f32x4 acc[S][2];
#pragma unroll
  for (int s = 0; s < S; ++s) { acc[s][0] = f32x4{0.f, 0.f, 0.f, 0.f}; acc[s][1] = f32x4{0.f, 0.f, 0.f, 0.f}; }
  const unsigned char* qp = cx.lds + LDS_Q + (grp * S + ((lane & 15) >> 1)) * QS_STRIDE + (lane & 1) * QH_STRIDE + (lane >> 4) * 16;
  u32x4 qf[8];
#pragma unroll
  for (int c = 0; c < 8; ++c) qf[c] = *reinterpret_cast<const u32x4*>(qp + c * 64);
  unsigned sel[8];
#pragma unroll
  for (int hf = 0; hf < 2; ++hf)
#pragma unroll
    for (int d = 0; d < 4; ++d) {
      const unsigned src = hf ? 0x0302u : 0x0100u;
      sel[hf * 4 + d] = (((lane & 7) >> 1) == d) ? ((lane & 1) ? (0x00000c0cu | (src << 16)) : (0x0c0c0000u | src)) : 0x0c0c0c0cu;
    }
  for (int k = kb; k < ke; ++k) {
    const bool more = (k + 1) < cx.nitems;
    const int nphys = __builtin_amdgcn_readlane(cx.plist, more ? k + 1 : k);
    f32x4 sc = f32x4{0.f, 0.f, 0.f, 0.f};
#pragma unroll
    for (int c = 0; c < 8; ++c) sc = mfma(r.k[c], qf[c], sc);
    if (!KVL || k + 1 < cx.nsh) { if (more) issue_k(r, cx.kbase, nphys); }
    else if (more) issue_kl(r, cx.kbase, cx.vbase, nphys);
    float t[4];
#pragma unroll
    for (int i = 0; i < 4; ++i) t[i] = sc[i] * C2;
    float pm = fmaxf(fmaxf(t[0], t[1]), fmaxf(t[2], t[3]));
    pm = xkg_max(pm);
    const float mn = fmaxf(m, pm);
    const float msub = (mn == -INFINITY) ? 0.f : mn;
    const float alpha = __builtin_amdgcn_exp2f(m - msub);
    m = mn;
    const unsigned p01 = cvtpk(__builtin_amdgcn_exp2f(t[0] - msub), __builtin_amdgcn_exp2f(t[1] - msub));
    const unsigned p23 = cvtpk(__builtin_amdgcn_exp2f(t[2] - msub), __builtin_amdgcn_exp2f(t[3] - msub));
    const float ps = (__uint_as_float(p01 << 16) + __uint_as_float(p01 & 0xffff0000u)) +
                     (__uint_as_float(p23 << 16) + __uint_as_float(p23 & 0xffff0000u));
    l = fmaf(l, alpha, ps);
    shared_pv<0>(cx, r, p01, p23, alpha, sel, acc[0][0], acc[0][1]);
    shared_pv<1>(cx, r, p01, p23, alpha, sel, acc[1][0], acc[1][1]);
    shared_pv<2>(cx, r, p01, p23, alpha, sel, acc[2][0], acc[2][1]);
    shared_pv<3>(cx, r, p01, p23, alpha, sel, acc[3][0], acc[3][1]);
    shared_pv<4>(cx, r, p01, p23, alpha, sel, acc[4][0], acc[4][1]);
    shared_pv<5>(cx, r, p01, p23, alpha, sel, acc[5][0], acc[5][1]);
    shared_pv<6>(cx, r, p01, p23, alpha, sel, acc[6][0], acc[6][1]);
    shared_pv<7>(cx, r, p01, p23, alpha, sel, acc[7][0], acc[7][1]);
    if (k + 1 < cx.nsh) issue_v<VSAUX>(r, cx.vbase, nphys);
    else if (more) issue_v<VAUX>(r, cx.vbase, nphys);
  }
  float* mrg = reinterpret_cast<float*>(cx.lds + LDS_MRG);
  const float lt = xkg_sum(l);
#pragma unroll
  for (int rd = 0; rd < S / MR; ++rd) {
#pragma unroll
    for (int ss = 0; ss < MR; ++ss) {
      float* rec = mrg + ((cx.wave * MR + ss) * 2 + (lane >> 5)) * MREC + 8 * (lane & 15) + 4 * ((lane >> 4) & 1);
      *reinterpret_cast<f32x4*>(rec) = acc[rd * MR + ss][0];
      *reinterpret_cast<f32x4*>(rec + 128) = acc[rd * MR + ss][1];
    }
    if ((lane >> 3) == rd) {
      float* rr = mrg + (cx.wave * MR * 2 + (lane & 7)) * MREC + 256;
      rr[0] = m; rr[1] = lt;
    }
    merge_rows<2 * MR>(cx, reinterpret_cast<float*>(cx.ws + WS_PO_SH),
                       ((size_t)(cx.head * NG + grp) * NWG + piece) * (2 * S) + rd * 2 * MR, grp * S + rd * MR, 2 * piece);
  }
}

__global__ void __launch_bounds__(NTHR, 2)
attn_main(const __hip_bfloat16* __restrict__ q, const __hip_bfloat16* __restrict__ kc, const __hip_bfloat16* __restrict__ vc,
          const int* __restrict__ bt, const int* __restrict__ ctx_lens, unsigned char* __restrict__ ws, unsigned long long kv_bytes) {
  __shared__ __attribute__((aligned(16))) unsigned char lds[LDS_BYTES];
  const int tid = threadIdx.x, lane = tid & 63, wave = __builtin_amdgcn_readfirstlane(tid >> 6);
  const int head = blockIdx.x & 7, wgi = blockIdx.x >> 3;
  int* smin = reinterpret_cast<int*>(lds + LDS_MISC);
  int2 tb[4];
#pragma unroll
  for (int i = 0; i < 4; ++i) tb[i] = *reinterpret_cast<const int2*>(bt + (wave * 4 + i) * NB + 2 * lane);
  const int2 z = *reinterpret_cast<const int2*>(bt + 2 * lane);
  const bool seqlane = lane >= NG && lane < NSEG;
  const int myctx = ctx_lens[seqlane ? lane - NG : 0];
  const int qrow = tid >> 3, qpc = tid & 7;
  const __hip_bfloat16* qsrc = q + ((size_t)(qrow >> 1) * H + head * 2 + (qrow & 1)) * D + qpc * 32;
  u32x4 qv[4];
#pragma unroll
  for (int i = 0; i < 4; ++i) qv[i] = *reinterpret_cast<const u32x4*>(qsrc + i * 8);
  asm volatile("" :: "s"(kc), "s"(vc), "s"(ws), "s"((unsigned)kv_bytes));
  const int nb_l = seqlane ? live_pages(myctx) : 0;
  const int pn_incl = wave_incl_scan(nb_l);
  const int fullmin = wave_min(seqlane ? (myctx <= 0 ? 0 : min(myctx, NB * BS) / BS) : NB);
  {
    bool ne = false, no = false;
#pragma unroll
    for (int i = 0; i < 4; ++i) {
      ne |= tb[i].x != z.x; no |= tb[i].y != z.y;
      *reinterpret_cast<int2*>(lds + LDS_BT + ((wave * 4 + i) * NB + 2 * lane) * 4) = tb[i];
    }
    const unsigned long long me = __ballot(ne), mo = __ballot(no);
    const int fe = me ? 2 * __builtin_ctzll(me) : NB, fo = mo ? 2 * __builtin_ctzll(mo) + 1 : NB;
    if (lane == 0) smin[wave] = min(fe, fo);
  }
  __syncthreads();
  int Pc = fullmin;
#pragma unroll
  for (int w = 0; w < NW; ++w) Pc = min(Pc, smin[w]);
  Pc = __builtin_amdgcn_readfirstlane(Pc);
  if (blockIdx.x == 0 && tid == 0) *reinterpret_cast<int*>(ws + WS_PLAN) = Pc;
  const Geo ge = seg_geometry(lane, nb_l, pn_incl, Pc);
  const int lo = (int)(((long long)ge.wtotal * wgi) / NWG), hi = (int)(((long long)ge.wtotal * (wgi + 1)) / NWG);
  const int pb = pos_to_page(lo, ge.wstart, ge.sh, ge.npg), pe = pos_to_page(hi, ge.wstart, ge.sh, ge.npg);
  const int cnt = pe - pb;
  unsigned long long segmask = __ballot(cnt > 0);
  int off = 0, total = 0;
  if (segmask) {
    const int jf = __builtin_ctzll(segmask), jl = 63 - __builtin_clzll(segmask);
    const int u_lo = __builtin_amdgcn_readlane(ge.ustart + pb, jf);
    off = ge.ustart + pb - u_lo;
    total = __builtin_amdgcn_readlane(ge.ustart + pe, jl) - u_lo;
  }
  const int g = wave + 8 * lane;
  int plist = 0;
  {
    unsigned long long mm = segmask;
    while (mm) {
      const int j = __builtin_ctzll(mm); mm &= mm - 1;
      const int o_ = __builtin_amdgcn_readlane(off, j), c_ = __builtin_amdgcn_readlane(cnt, j), b_ = __builtin_amdgcn_readlane(pb, j);
      const int row = j < NG ? j * S : j - NG;
      const int base = j < NG ? 0 : Pc;
      if (g >= o_ && g < o_ + c_) plist = reinterpret_cast<const int*>(lds + LDS_BT)[row * NB + base + b_ + (g - o_)];
    }
  }
  Ctx cx;
  cx.lane = lane; cx.wave = wave; cx.head = head; cx.wgi = wgi; cx.lds = lds; cx.ws = ws; cx.plist = plist;
  cx.nitems = __builtin_amdgcn_readfirstlane(total > wave ? (total - wave + 7) >> 3 : 0);
  {
    int shcnt = 0;
#pragma unroll
    for (int j = 0; j < NG; ++j) shcnt += __builtin_amdgcn_readlane(cnt, j);
    cx.nsh = shcnt > wave ? (shcnt - wave + 7) >> 3 : 0;
  }
  cx.kbase.rs = __builtin_amdgcn_make_buffer_rsrc(const_cast<__hip_bfloat16*>(kc), 0, (unsigned)kv_bytes, 0x00020000);
  cx.vbase.rs = __builtin_amdgcn_make_buffer_rsrc(const_cast<__hip_bfloat16*>(vc), 0, (unsigned)kv_bytes, 0x00020000);
  cx.kbase.off = (head * D + (lane & 15) * TOK_STRIDE + (lane >> 4) * 8) * 2;
  cx.vbase.off = (head * D + (lane >> 4) * 4 * TOK_STRIDE + (lane & 15) * 8) * 2;
#pragma unroll
  for (int d = 0; d < 4; ++d) cx.mk[d] = (((lane & 7) >> 1) == d) ? ((lane & 1) ? 0xffff0000u : 0x0000ffffu) : 0u;
  KV r;
  if (cx.nitems > 0) {
    const int ph = __builtin_amdgcn_readlane(plist, 0);
    if (!KVL || cx.nsh > 0) issue_k(r, cx.kbase, ph); else issue_kl(r, cx.kbase, cx.vbase, ph);
    if (cx.nsh > 0) issue_v<VSAUX>(r, cx.vbase, ph); else issue_v<VAUX>(r, cx.vbase, ph);
  } else {
#pragma unroll
    for (int i = 0; i < 8; ++i) { r.k[i] = u32x4{0u, 0u, 0u, 0u}; r.v[i] = u32x4{0u, 0u, 0u, 0u}; }
  }
  {
    unsigned char* qd = lds + LDS_Q + (qrow >> 1) * QS_STRIDE + (qrow & 1) * QH_STRIDE + qpc * 64;
#pragma unroll
    for (int i = 0; i < 4; ++i) *reinterpret_cast<u32x4*>(qd + i * 16) = qv[i];
  }
  const int lo_l = (int)(((long long)ge.wtotal * (lane & 31)) / NWG), hi_l = (int)(((long long)ge.wtotal * ((lane & 31) + 1)) / NWG);
  __syncthreads();
  while (segmask) {
    const int j = __builtin_ctzll(segmask); segmask &= segmask - 1;
    const int o_ = __builtin_amdgcn_readlane(off, j), c_ = __builtin_amdgcn_readlane(cnt, j), b_ = __builtin_amdgcn_readlane(pb, j);
    const int kb = (o_ - wave + 7) >> 3, ke = (o_ + c_ - wave + 7) >> 3;
    const int pgrel = b_ + (wave + 8 * kb - o_);
    const int j_ws = __builtin_amdgcn_readlane(ge.wstart, j), j_np = __builtin_amdgcn_readlane(ge.npg, j);
    const bool has_l = pos_to_page(hi_l, j_ws, j < NG, j_np) > pos_to_page(lo_l, j_ws, j < NG, j_np);
    const int piece = __builtin_popcountll(__ballot(has_l && lane < wgi));
    if (j < NG) {
      run_shared(cx, r, j, kb, ke, piece);
    } else {
      const int cl = __builtin_amdgcn_readlane(myctx, j);
      run_private(cx, r, j - NG, kb, ke, Pc + pgrel, min(cl, NB * BS), piece);
    }
  }
}

constexpr int SPEC = 3, SPECP = 2, SPECML = 8;
__global__ void __launch_bounds__(256)
attn_combine(const int* __restrict__ ctx_lens, const unsigned char* __restrict__ ws, __hip_bfloat16* __restrict__ out) {
  const int lane = threadIdx.x & 63, wave = threadIdx.x >> 6;
#define CMAP 1
  const int rowid = CMAP ? (((blockIdx.x >> 3) * 2 + (wave >> 1)) * H + (blockIdx.x & 7) * 2 + (wave & 1)) : (blockIdx.x * 4 + wave);
  const int b = __builtin_amdgcn_readfirstlane(rowid >> 4), h = rowid & 15, head = h >> 1, hh = h & 1;
  const bool seqlane = lane >= NG && lane < NSEG;
  const int myctx = *(seqlane ? ctx_lens + (lane - NG) : reinterpret_cast<const int*>(ws + WS_PLAN));
  const bool sh = lane < 32;
  const size_t sl_sh = (size_t)(head * NG + b / S) * NWG * (2 * S) + ((b % S) * 2 + hh);
  const size_t sl_pr = (size_t)(head * B + b) * NWG * 2 + hh;
  const float2* mlp = reinterpret_cast<const float2*>(ws + WS_ML) + rowid * (2 * NWG) + 2 * (lane & 31) + (sh ? 0 : 1);
  float2 mlraw = make_float2(0.f, 0.f);
  if ((lane & 31) < SPECML) mlraw = *mlp;
  const float* po_sh = reinterpret_cast<const float*>(ws + WS_PO_SH) + sl_sh * D + 4 * lane;
  const float* po_pr = reinterpret_cast<const float*>(ws + WS_PO_PR) + sl_pr * D + 4 * lane;
  f32x4 vs[SPEC], vp[SPECP];
#pragma unroll
  for (int i = 0; i < SPEC; ++i) {
    vs[i] = *reinterpret_cast<const f32x4*>(po_sh + (size_t)i * (2 * S) * D);
    if (i < SPECP) vp[i] = *reinterpret_cast<const f32x4*>(po_pr + (size_t)i * 2 * D);
  }
  const int Pc = __builtin_amdgcn_readlane(myctx, 63);
  const int nb_l = seqlane ? live_pages(myctx) : 0;
  const Geo ge = seg_geometry(lane, nb_l, wave_incl_scan(nb_l), Pc);
  const int wgi = lane & 31;
  const int s_npg = sh ? Pc : __builtin_amdgcn_readlane(ge.npg, NG + b);
  const int s_ws = sh ? __builtin_amdgcn_readlane(ge.wstart, b / S) : __builtin_amdgcn_readlane(ge.wstart, NG + b);
  const int lo = (int)(((long long)ge.wtotal * wgi) / NWG), hi = (int)(((long long)ge.wtotal * (wgi + 1)) / NWG);
  const bool has = pos_to_page(hi, s_ws, sh, s_npg) > pos_to_page(lo, s_ws, sh, s_npg);
  const unsigned long long hm = __ballot(has);
  const int n_sh = __builtin_popcount((unsigned)hm), n_pr = __builtin_popcount((unsigned)(hm >> 32));
  const bool valid = sh ? (lane < n_sh) : (lane - 32 < n_pr);
  if (valid && (lane & 31) >= SPECML) mlraw = *mlp;
  const float mv = valid ? mlraw.x : -INFINITY, lv = valid ? mlraw.y : 0.f;
  const float M = wave_fmax(mv);
  const float w = (lv > 0.f) ? __builtin_amdgcn_exp2f(mv - M) : 0.f;
  const float L = wave_fsum(w * lv);
  const int wi = __builtin_bit_cast(int, w);
  f32x4 o = f32x4{0.f, 0.f, 0.f, 0.f};
#pragma unroll
  for (int i = 0; i < SPEC; ++i) {
    if (i < n_sh) o += vs[i] * __builtin_bit_cast(float, __builtin_amdgcn_readlane(wi, i));
    if (i < SPECP && i < n_pr) o += vp[i < SPECP ? i : 0] * __builtin_bit_cast(float, __builtin_amdgcn_readlane(wi, 32 + i));
  }
  for (int i = SPEC; i < n_sh; ++i)
    o += *reinterpret_cast<const f32x4*>(po_sh + (size_t)i * (2 * S) * D) * __builtin_bit_cast(float, __builtin_amdgcn_readlane(wi, i));
  for (int i = SPECP; i < n_pr; ++i)
    o += *reinterpret_cast<const f32x4*>(po_pr + (size_t)i * 2 * D) * __builtin_bit_cast(float, __builtin_amdgcn_readlane(wi, 32 + i));
  const float inv = (L > 0.f) ? 1.f / L : 0.f;
  uint2 pk;
  pk.x = cvtpk(o[0] * inv, o[1] * inv);
  pk.y = cvtpk(o[2] * inv, o[3] * inv);
  *reinterpret_cast<uint2*>(out + (size_t)rowid * D + 4 * lane) = pk;
}
}

extern "C" void kernel_launch(void* const* d_in, const int* in_sizes, int n_in, void* d_out, int out_size, void* d_ws,
                              size_t ws_size, hipStream_t stream) {
  const __hip_bfloat16* q = (const __hip_bfloat16*)d_in[0];
  const __hip_bfloat16* kc = (const __hip_bfloat16*)d_in[1];
  const __hip_bfloat16* vc = (const __hip_bfloat16*)d_in[2];
  const int* bt = (const int*)d_in[3];
  const int* ctx = (const int*)d_in[4];
  attn_main<<<HKV * NWG, NTHR, 0, stream>>>(q, kc, vc, bt, ctx, (unsigned char*)d_ws, (unsigned long long)in_sizes[1] * 2ull);
  attn_combine<<<B * H / 4, 256, 0, stream>>>(ctx, (const unsigned char*)d_ws, (__hip_bfloat16*)d_out);
}
```
